```python
import torch

def _softmax(x, axis=-1):
    return torch.softmax(x, dim=axis)
def _gelu(x):
    return torch.nn.functional.gelu(x, approximate="tanh")
def _silu(x):
    return torch.nn.functional.silu(x)
def _sigmoid(x):
    return torch.sigmoid(x)
def _layernorm(x, axis=-1, eps=1e-5):
    m = x.mean(dim=axis, keepdim=True)
    v = x.var(dim=axis, keepdim=True, unbiased=False)
    return (x - m) / torch.sqrt(v + eps)
def _scatter_add(tgt, idx, src, axis=0):
    return tgt.clone().index_add_(axis, idx, src)
def _segment_sum(src, seg, n):
    out = torch.zeros((n,) + src.shape[1:], dtype=src.dtype)
    return out.index_add_(0, seg, src)
def _pad(x, pw):
    flat = tuple(v for pair in reversed(pw) for v in pair)
    return torch.nn.functional.pad(x, flat)

kernel_name = 'sequential_preLN__spectral_fnet__moe_arch_mod__prompt'

def setup_inputs():

    _w = (lambda a: (lambda t: t.to(torch.bfloat16) if t.is_floating_point() else t)(torch.as_tensor(a)))
    def _A_setup():
        B, S, D = 1, 2048, 576
        ns = torch.arange(S)[:, None] * torch.arange(S)[None, :]
        nd = torch.arange(D)[:, None] * torch.arange(D)[None, :]
        Ws_re = torch.cos(-2.0 * torch.pi * ns / S).to(float)
        Ws_im = torch.sin(-2.0 * torch.pi * ns / S).to(float)
        Wd_re = torch.cos(-2.0 * torch.pi * nd / D).to(float)
        Wd_im = torch.sin(-2.0 * torch.pi * nd / D).to(float)
        return {
            "x": _w((lambda *s: torch.randn(*s, dtype=torch.bfloat16) * 0.05)(B, S, D)),
            "dft_seq_re": _w(Ws_re),
            "dft_seq_im": _w(Ws_im),
            "dft_hid_re": _w(Wd_re),
            "dft_hid_im": _w(Wd_im),
        }
    _A_in = _A_setup()

    def _B_setup():
        B, S, D, F, C = 1, 2048, 576, 4608, 1
        return {
            "x": _w((lambda *s: torch.randn(*s, dtype=torch.bfloat16) * 0.05)(B, S, D)),
            "w_r": _w((lambda *s: torch.randn(*s, dtype=torch.bfloat16) * 1.6)(D)),
            "w_gate": _w((lambda *s: torch.randn(*s, dtype=torch.bfloat16) * 0.4)(D, F)),
            "w_up": _w((lambda *s: torch.randn(*s, dtype=torch.bfloat16) * 0.4)(D, F)),
            "w_down": _w((lambda *s: torch.randn(*s, dtype=torch.bfloat16) * 0.4)(F, D)),
        }
    _B_in = _B_setup()

    return {
        "A_x": _A_in["x"],
        "A_dft_seq_re": _A_in["dft_seq_re"],
        "A_dft_seq_im": _A_in["dft_seq_im"],
        "A_dft_hid_re": _A_in["dft_hid_re"],
        "A_dft_hid_im": _A_in["dft_hid_im"],
        "B_x": _B_in["x"],
        "B_w_r": _B_in["w_r"],
        "B_w_gate": _B_in["w_gate"],
        "B_w_up": _B_in["w_up"],
        "B_w_down": _B_in["w_down"],
    }

def reference(A_x, A_dft_seq_re, A_dft_seq_im, A_dft_hid_re, A_dft_hid_im, B_x, B_w_r, B_w_gate, B_w_up, B_w_down):
    def _A_ref(x, dft_seq_re, dft_seq_im, dft_hid_re, dft_hid_im):
        a = torch.einsum("bsd,ed->bse", x, dft_hid_re)
        b = torch.einsum("bsd,ed->bse", x, dft_hid_im)
        y = torch.einsum("ts,bse->bte", dft_seq_re, a) - torch.einsum("ts,bse->bte", dft_seq_im, b)
        return y

    def _B_ref(x, w_r, w_gate, w_up, w_down):
        B, S, D = x.shape
        C = 1
        r = x @ w_r
        idx = torch.argsort(-r, axis=-1)[:, :C]
        idx_e = (lambda x, n, axis: torch.repeat_interleave(x, n, dim=axis))(idx.reshape(B, C, 1), D, axis=2)
        x_sel = (lambda x, i, axis: torch.take_along_dim(x, i, dim=axis))(x, idx_e, axis=1)
        g = _silu(x_sel @ w_gate)
        u = x_sel @ w_up
        y = (g * u) @ w_down
        r_sel = _sigmoid((lambda x, i, axis: torch.take_along_dim(x, i, dim=axis))(r, idx, axis=1))
        y = y * r_sel.reshape(B, C, 1)
        b_off = (torch.arange(B) * S).reshape(B, 1)
        flat_idx = (idx + b_off).reshape(B * C)
        zero = (x * 0.0).reshape(B * S, D)
        upd = _scatter_add(zero, flat_idx, y.reshape(B * C, D), axis=0)
        return x + upd.reshape(B, S, D)

    x = A_x
    h = x + _A_ref(_layernorm(x, axis=-1), A_dft_seq_re, A_dft_seq_im, A_dft_hid_re, A_dft_hid_im)
    h2 = h + _B_ref(_layernorm(h, axis=-1), B_w_r, B_w_gate, B_w_up, B_w_down)
    return h2
```

```cpp
#include <hip/hip_runtime.h>
#include <hip/hip_bf16.h>
#include <stdint.h>

typedef short bf16x8 __attribute__((ext_vector_type(8)));
typedef float f32x4 __attribute__((ext_vector_type(4)));
typedef unsigned v4u __attribute__((ext_vector_type(4)));
typedef unsigned v2u __attribute__((ext_vector_type(2)));

constexpr int S = 2048, D = 576, F = 4608;

__device__ __forceinline__ float bf2f(unsigned short b) { return __builtin_bit_cast(float, (unsigned)b << 16); }
__device__ __forceinline__ unsigned short f2bf(float f) {
  __hip_bfloat16 h = __float2bfloat16(f);
  return __builtin_bit_cast(unsigned short, h);
}
__device__ __forceinline__ float wave_sum(float v) {
  for (int o = 32; o > 0; o >>= 1) v += __shfl_xor(v, o);
  return v;
}

__global__ void k_ln1(const unsigned short* __restrict__ x, unsigned short* __restrict__ xn, unsigned long long* best) {
  int row = blockIdx.x * 4 + (threadIdx.x >> 6), lane = threadIdx.x & 63;
  if (blockIdx.x == 0 && threadIdx.x == 0) *best = 0ull;
  float v[9]; float s = 0;
  for (int i = 0; i < 9; ++i) { v[i] = bf2f(x[row * D + lane + 64 * i]); s += v[i]; }
  float m = wave_sum(s) * (1.f / D);
  float q = 0;
  for (int i = 0; i < 9; ++i) { float d = v[i] - m; q += d * d; }
  float var = wave_sum(q) * (1.f / D);
  float rs = 1.0f / sqrtf(var + 1e-5f);
  for (int i = 0; i < 9; ++i) xn[row * D + lane + 64 * i] = f2bf((v[i] - m) * rs);
}

__global__ void k_gemm1(const unsigned short* __restrict__ xn, const unsigned short* __restrict__ Dr,
                        const unsigned short* __restrict__ Di, unsigned short* __restrict__ abT) {
  int wave = blockIdx.x * 4 + (threadIdx.x >> 6), lane = threadIdx.x & 63;
  int st = wave / 72, et = wave % 72, p = et / 36, e0 = (et % 36) * 16, s0 = st * 16;
  const unsigned short* Dp = p ? Di : Dr;
  f32x4 acc = {0, 0, 0, 0};
  for (int k = 0; k < D; k += 32) {
    bf16x8 a = *(const bf16x8*)(xn + (s0 + (lane & 15)) * D + k + 8 * (lane >> 4));
    bf16x8 b = *(const bf16x8*)(Dp + (e0 + (lane & 15)) * D + k + 8 * (lane >> 4));
    acc = __builtin_amdgcn_mfma_f32_16x16x32_bf16(a, b, acc, 0, 0, 0);
  }
  v2u o;
  o.x = f2bf(acc[0]) | ((unsigned)f2bf(acc[1]) << 16);
  o.y = f2bf(acc[2]) | ((unsigned)f2bf(acc[3]) << 16);
  *(v2u*)(abT + ((size_t)p * D + e0 + (lane & 15)) * S + s0 + 4 * (lane >> 4)) = o;
}

__global__ void k_gemm2(const unsigned short* __restrict__ Cr, const unsigned short* __restrict__ Ci,
                        const unsigned short* __restrict__ abT, const unsigned short* __restrict__ x, float* __restrict__ h) {
  int wave = blockIdx.x * 4 + (threadIdx.x >> 6), lane = threadIdx.x & 63;
  int tt = wave / 12, et = wave % 12, t0 = tt * 32, e0 = et * 48;
  f32x4 accr[2][3] = {}, acci[2][3] = {};
  for (int k = 0; k < S; k += 32) {
    bf16x8 ar[3], ai[3], br[2], bi[2];
    for (int i = 0; i < 3; ++i) {
      ar[i] = *(const bf16x8*)(abT + ((size_t)(e0 + 16 * i + (lane & 15))) * S + k + 8 * (lane >> 4));
      ai[i] = *(const bf16x8*)(abT + ((size_t)(D + e0 + 16 * i + (lane & 15))) * S + k + 8 * (lane >> 4));
    }
    for (int j = 0; j < 2; ++j) {
      br[j] = *(const bf16x8*)(Cr + (size_t)(t0 + 16 * j + (lane & 15)) * S + k + 8 * (lane >> 4));
      bi[j] = *(const bf16x8*)(Ci + (size_t)(t0 + 16 * j + (lane & 15)) * S + k + 8 * (lane >> 4));
    }
    for (int j = 0; j < 2; ++j)
      for (int i = 0; i < 3; ++i) {
        accr[j][i] = __builtin_amdgcn_mfma_f32_16x16x32_bf16(ar[i], br[j], accr[j][i], 0, 0, 0);
        acci[j][i] = __builtin_amdgcn_mfma_f32_16x16x32_bf16(ai[i], bi[j], acci[j][i], 0, 0, 0);
      }
  }
  for (int j = 0; j < 2; ++j)
    for (int i = 0; i < 3; ++i) {
      int t = t0 + 16 * j + (lane & 15), e = e0 + 16 * i + 4 * (lane >> 4);
      f32x4 o;
      for (int r = 0; r < 4; ++r) o[r] = bf2f(x[t * D + e + r]) + (accr[j][i][r] - acci[j][i][r]);
      *(f32x4*)(h + (size_t)t * D + e) = o;
    }
}

__global__ void k_ln2(const float* __restrict__ h, const unsigned short* __restrict__ wr, unsigned short* __restrict__ out,
                      float* __restrict__ hn_out, unsigned long long* best) {
  int row = blockIdx.x * 4 + (threadIdx.x >> 6), lane = threadIdx.x & 63;
  float v[9]; float s = 0;
  for (int i = 0; i < 9; ++i) { v[i] = bf2f(f2bf(h[row * D + lane + 64 * i])); s += v[i]; }
  float m = wave_sum(s) * (1.f / D);
  float q = 0;
  for (int i = 0; i < 9; ++i) { float d = v[i] - m; q += d * d; }
  float var = wave_sum(q) * (1.f / D);
  float rs = 1.0f / sqrtf(var + 1e-5f);
  float r = 0;
  for (int i = 0; i < 9; ++i) {
    float hn = bf2f(f2bf((v[i] - m) * rs));
    r += hn * bf2f(wr[lane + 64 * i]);
    hn_out[row * D + lane + 64 * i] = hn;
    out[row * D + lane + 64 * i] = f2bf(v[i] + hn);
  }
  r = wave_sum(r);
  if (lane == 0) {
    unsigned u = __builtin_bit_cast(unsigned, r);
    u = (u & 0x80000000u) ? ~u : (u | 0x80000000u);
    unsigned long long key = ((unsigned long long)u << 32) | (unsigned)(S - 1 - row);
    atomicMax(best, key);
  }
}

__global__ void k_ffn1(const float* __restrict__ hn, const unsigned long long* __restrict__ best,
                       const unsigned short* __restrict__ wg, const unsigned short* __restrict__ wu, float* __restrict__ gu) {
  int f = blockIdx.x * 64 + (threadIdx.x & 63), part = threadIdx.x >> 6;
  __shared__ float sg[4][64], su[4][64];
  int row = S - 1 - (int)(unsigned)(*best & 0xffffffffu);
  float g = 0, u = 0;
  for (int d = part * 144; d < part * 144 + 144; ++d) {
    float xv = hn[row * D + d];
    g += xv * bf2f(wg[(size_t)d * F + f]);
    u += xv * bf2f(wu[(size_t)d * F + f]);
  }
  sg[part][threadIdx.x & 63] = g; su[part][threadIdx.x & 63] = u;
  __syncthreads();
  if (part == 0) {
    int l = threadIdx.x;
    g = sg[0][l] + sg[1][l] + sg[2][l] + sg[3][l];
    u = su[0][l] + su[1][l] + su[2][l] + su[3][l];
    float sl = g / (1.f + __expf(-g));
    gu[f] = sl * u;
  }
}

__global__ void k_ffn2(const float* __restrict__ h, const float* __restrict__ hn, const unsigned long long* __restrict__ best,
                       const float* __restrict__ gu, const unsigned short* __restrict__ wd, unsigned short* __restrict__ out) {
  int e = blockIdx.x * 64 + (threadIdx.x & 63), part = threadIdx.x >> 6;
  __shared__ float sy[16][64];
  unsigned long long key = *best;
  int row = S - 1 - (int)(unsigned)(key & 0xffffffffu);
  unsigned u = (unsigned)(key >> 32);
  u = (u & 0x80000000u) ? (u & 0x7fffffffu) : ~u;
  float r = __builtin_bit_cast(float, u);
  float y = 0;
  for (int f = part * 288; f < part * 288 + 288; ++f) y += gu[f] * bf2f(wd[(size_t)f * D + e]);
  sy[part][threadIdx.x & 63] = y;
  __syncthreads();
  if (part == 0) {
    y = 0;
    for (int i = 0; i < 16; ++i) y += sy[i][threadIdx.x];
    float sg = 1.f / (1.f + __expf(-r));
    float hv = bf2f(f2bf(h[row * D + e]));
    out[row * D + e] = f2bf(hv + (hn[row * D + e] + y * sg));
  }
}

extern "C" void kernel_launch(void* const* d_in, const int* in_sizes, int n_in, void* d_out, int out_size, void* d_ws,
                              size_t ws_size, hipStream_t stream) {
  const unsigned short* x = (const unsigned short*)d_in[0];
  const unsigned short* Cr = (const unsigned short*)d_in[1];
  const unsigned short* Ci = (const unsigned short*)d_in[2];
  const unsigned short* Dr = (const unsigned short*)d_in[3];
  const unsigned short* Di = (const unsigned short*)d_in[4];
  const unsigned short* wr = (const unsigned short*)d_in[6];
  const unsigned short* wg = (const unsigned short*)d_in[7];
  const unsigned short* wu = (const unsigned short*)d_in[8];
  const unsigned short* wd = (const unsigned short*)d_in[9];
  unsigned short* out = (unsigned short*)d_out;
  char* ws = (char*)d_ws;
  unsigned long long* best = (unsigned long long*)ws;
  float* gu = (float*)(ws + 4096);
  unsigned short* xn = (unsigned short*)(ws + (1 << 20));
  unsigned short* abT = (unsigned short*)(ws + (4 << 20));
  float* h = (float*)(ws + (12 << 20));
  float* hn = (float*)(ws + (20 << 20));
  k_ln1<<<S / 4, 256, 0, stream>>>(x, xn, best);
  k_gemm1<<<128 * 72 / 4, 256, 0, stream>>>(xn, Dr, Di, abT);
  k_gemm2<<<64 * 12 / 4, 256, 0, stream>>>(Cr, Ci, abT, x, h);
  k_ln2<<<S / 4, 256, 0, stream>>>(h, wr, out, hn, best);
  k_ffn1<<<F / 64, 256, 0, stream>>>(hn, best, wg, wu, gu);
  k_ffn2<<<D / 64, 1024, 0, stream>>>(h, hn, best, gu, wd, out);
}
```

```cpp
#include <hip/hip_runtime.h>
#include <hip/hip_bf16.h>
#include <stdint.h>

#define LAS __attribute__((address_space(3)))
#define GAS __attribute__((address_space(1)))
typedef short bf16x8 __attribute__((ext_vector_type(8)));
typedef __bf16 bf16x2 __attribute__((ext_vector_type(2)));
typedef float f32x4 __attribute__((ext_vector_type(4)));
typedef unsigned v4u __attribute__((ext_vector_type(4)));
typedef unsigned v2u __attribute__((ext_vector_type(2)));
typedef unsigned long long u64;
typedef __amdgpu_buffer_rsrc_t rsrc_t;

constexpr int S = 2048, D = 576, F = 4608;
constexpr int AUX_SC1 = 16;
constexpr size_t WS_GRAN = 0;
constexpr size_t WS_CAND = 4096;
constexpr size_t WS_YG = WS_CAND + 256 * 1152;
constexpr size_t WS_BF = 1 << 20;
constexpr size_t WS_PART = WS_BF + 256 * 18432;
constexpr size_t WS_END = WS_PART + 256 * 16 * 2304;
constexpr int L_BR = 0, BSLOT = 18432, NBS = 4;
constexpr int L_AR = NBS * BSLOT, ASLOT = 16384, NAS = 5;
constexpr int L_MISC = L_AR + NAS * ASLOT;
constexpr int L_DBG = L_MISC + 2048;
constexpr int LDS_BYTES = L_MISC + 4096;

struct Params {
  const char *x, *Cr, *Ci, *Dr, *Di, *wr, *wg, *wu, *wd;
  char* out;
  char* ws;
};

__device__ __forceinline__ float bf2f(unsigned b) { return __builtin_bit_cast(float, b << 16); }
__device__ __forceinline__ float bfhi(unsigned b) { return __builtin_bit_cast(float, b & 0xffff0000u); }
__device__ __forceinline__ unsigned pack2(float lo, float hi) {
  bf16x2 v; v.x = (__bf16)lo; v.y = (__bf16)hi;
  return __builtin_bit_cast(unsigned, v);
}
__device__ __forceinline__ float dot2(unsigned a, unsigned b, float c) {
  return __builtin_amdgcn_fdot2_f32_bf16(__builtin_bit_cast(bf16x2, a), __builtin_bit_cast(bf16x2, b), c, false);
}
__device__ __forceinline__ f32x4 mfma(v4u a, v4u b, f32x4 c) {
  return __builtin_amdgcn_mfma_f32_16x16x32_bf16(__builtin_bit_cast(bf16x8, a), __builtin_bit_cast(bf16x8, b), c, 0, 0, 0);
}
__device__ __forceinline__ rsrc_t mkrs(const void* p, unsigned bytes) { return __builtin_amdgcn_make_buffer_rsrc((void*)p, 0, bytes, 0x00020000); }
template <int AUX> __device__ __forceinline__ void dma(rsrc_t rs, LAS char* dst, int voff, int soff) {
  __builtin_amdgcn_raw_ptr_buffer_load_lds(rs, dst, 16, voff, soff, 0, AUX);
}
#define BAR() asm volatile("s_barrier" ::: "memory")
#define WAITV(n) asm volatile("s_waitcnt vmcnt(%0)" ::"n"(n) : "memory")
#define WAITL0() asm volatile("s_waitcnt lgkmcnt(0)" ::: "memory")
#define LDSV4(p) (*(LAS v4u*)(p))
#define STAMP(kid, i)
#define STAMPC(kid, i)
#define STAMP_INIT()
#define STAMP_FLUSH(kid)

__global__ __launch_bounds__(512, 2) void k1(Params p) {
  extern __shared__ __attribute__((aligned(16))) char smem[];
  LAS char* L = (LAS char*)smem;
  const int b = blockIdx.x, tid = threadIdx.x, l = tid & 63, w = __builtin_amdgcn_readfirstlane(tid >> 6);
  const int xcd = b & 7, jj = b >> 3, q = xcd >> 1, m = (xcd & 1) * 8 + (jj >> 2), n = jj & 3;
  const int part = q >> 1, s0 = ((q & 1) * 16 + m) * 64;
  STAMP_INIT(); STAMP(0, 0);
  {
    GAS u64* yg = (GAS u64*)(p.ws + WS_YG);
    if (tid < 324) yg[b * 324 + tid] = 0ull;
    if (tid == 0) ((GAS u64*)(p.ws + WS_GRAN))[b] = 0ull;
  }
  if (w >= 4) {
    const int lw = w - 4;
    const int rperm = 8 * (l >> 5) + ((l >> 3) & 3), rlin = l >> 3;
    const int c0 = ((l & 7) ^ (l >> 4)) * 16, c1 = ((l & 7) ^ (4 + (l >> 4))) * 16;
    const int vp0 = rperm * 1152 + c0, vp1 = rperm * 1152 + c1;
    if (lw < 2) {
      rsrc_t rs = mkrs(part ? p.Di : p.Dr, D * D * 2);
      int vk[9], sk[9];
#pragma unroll
      for (int k = 0; k < 9; ++k) {
        const int P = lw * 9 + k, j = P >> 1, odd = P & 1;
        const int sc = j < 8 ? 32 * (j >> 1) + 4 * (j & 1) + 16 * odd : 128 + 8 * odd;
        sk[k] = (n * 144 + sc) * 1152;
        vk[k] = (j < 8 ? rperm : rlin) * 1152 + (odd ? c1 : c0);
      }
      auto issue = [=](int st) {
        LAS char* dst = L + L_BR + (st & 3) * BSLOT + lw * 9216;
#pragma unroll
        for (int k = 0; k < 9; ++k) dma<0>(rs, dst + k * 1024, vk[k], sk[k] + st * 128);
      };
      issue(0); issue(1); issue(2); issue(3);
      WAITV(27); BAR();
      for (int j = 1; j < 9; ++j) {
        if (j >= 2 && j + 2 < 9) issue(j + 2);
        if (j <= 6) WAITV(18); else if (j == 7) WAITV(9); else WAITV(0);
        BAR();
      }
    } else {
      rsrc_t rs = mkrs(p.x, S * D * 2);
#pragma unroll
      for (int st = 0; st < 9; ++st) {
#pragma unroll
        for (int k = 0; k < 4; ++k) {
          const int P = (lw - 2) * 4 + k, t = P >> 1, odd = P & 1;
          const int sc = 32 * (t >> 1) + 4 * (t & 1) + 16 * odd;
          dma<0>(rs, L + L_AR + st * 8192 + P * 1024, odd ? vp1 : vp0, (s0 + sc) * 1152 + st * 128);
        }
      }
      WAITV(32); BAR(); WAITV(28); BAR(); WAITV(24); BAR(); WAITV(20); BAR(); WAITV(16); BAR();
      WAITV(12); BAR(); WAITV(8); BAR(); WAITV(4); BAR(); WAITV(0); BAR();
    }
    BAR();
    STAMP_FLUSH(0);
    return;
  }
  const int ws_ = w >> 1, wn = w & 1;
  const int o0 = (l & 15) * 128 + (((l >> 4) ^ ((l & 15) >> 1)) * 16), o1 = o0 ^ 64;
  f32x4 acc[2][4], acc8 = {0, 0, 0, 0};
#pragma unroll
  for (int t = 0; t < 2; ++t)
#pragma unroll
    for (int j = 0; j < 4; ++j) acc[t][j] = f32x4{0, 0, 0, 0};
  float s1 = 0, s2 = 0, cs0 = 0, cs1 = 0, cs8 = 0;
  const unsigned ONES = 0x3f803f80u;
  auto step = [&](LAS char* xb, LAS char* db, int o) {
    v4u xf[2], df[5];
#pragma unroll
    for (int t = 0; t < 2; ++t) xf[t] = LDSV4(xb + (2 * ws_ + (t ^ wn)) * 2048 + o);
#pragma unroll
    for (int j = 0; j < 4; ++j) df[j] = LDSV4(db + (4 * wn + (j ^ (2 * ws_))) * 2048 + o);
    df[4] = LDSV4(db + 8 * 2048 + o);
#pragma unroll
    for (int t = 0; t < 2; ++t)
#pragma unroll
      for (int j = 0; j < 4; ++j) acc[t][j] = mfma(xf[t], df[j], acc[t][j]);
    const v4u xs = xf[0];
    acc8 = mfma(xs, df[4], acc8);
    const v4u d0 = df[0], d1 = df[1];
#pragma unroll
    for (int i = 0; i < 4; ++i) {
      const unsigned xv = xs[i], a = d0[i], bb = d1[i], c = df[4][i];
      s1 = dot2(xv, ONES, s1); s2 = dot2(xv, xv, s2);
      cs0 = dot2(a, ONES, cs0); cs1 = dot2(bb, ONES, cs1); cs8 = dot2(c, ONES, cs8);
    }
  };
#pragma unroll 1
  for (int st = 0; st < 9; ++st) {
    BAR();
    STAMP(0, 1 + st);
    LAS char* xb = L + L_AR + st * 8192;
    LAS char* db = L + L_BR + (st & 3) * BSLOT;
    step(xb, db, o0);
    step(xb, db, o1);
    WAITL0();
  }
  STAMP(0, 10);
  s1 += __shfl_xor(s1, 16); s1 += __shfl_xor(s1, 32);
  s2 += __shfl_xor(s2, 16); s2 += __shfl_xor(s2, 32);
  cs0 += __shfl_xor(cs0, 16); cs0 += __shfl_xor(cs0, 32);
  cs1 += __shfl_xor(cs1, 16); cs1 += __shfl_xor(cs1, 32);
  cs8 += __shfl_xor(cs8, 16); cs8 += __shfl_xor(cs8, 32);
  LAS float* smean = (LAS float*)(L + L_MISC);
  LAS float* srstd = smean + 64;
  LAS float* scs = smean + 128;
  if (l < 16) {
    const float mean = s1 * (1.f / D);
    const float var = fmaxf(s2 * (1.f / D) - mean * mean, 0.f);
    float rs = 1.0f / sqrtf(var + 1e-5f);
    if (part) rs = -rs;
    smean[(2 * ws_ + wn) * 16 + l] = mean;
    srstd[(2 * ws_ + wn) * 16 + l] = rs;
    scs[(4 * wn + 2 * ws_) * 16 + l] = cs0;
    scs[(4 * wn + 2 * ws_ + 1) * 16 + l] = cs1;
  }
  WAITL0(); BAR();
  f32x4 mean[2], rstd[2];
#pragma unroll
  for (int t = 0; t < 2; ++t) {
    mean[t] = *(LAS f32x4*)(smean + (2 * ws_ + (t ^ wn)) * 16 + 4 * (l >> 4));
    rstd[t] = *(LAS f32x4*)(srstd + (2 * ws_ + (t ^ wn)) * 16 + 4 * (l >> 4));
  }
  GAS char* ob = (GAS char*)p.ws + WS_BF + (size_t)((q * 4 + n) * 16 + m) * BSLOT + ws_ * 9216 + l * 16;
#pragma unroll
  for (int j = 0; j < 4; ++j) {
    const int jg = 4 * wn + (j ^ (2 * ws_));
    const float c = scs[jg * 16 + (l & 15)];
    f32x4 v0 = (acc[0][j] - mean[0] * c) * rstd[0], v1 = (acc[1][j] - mean[1] * c) * rstd[1];
    const unsigned p0 = pack2(v0[0], v0[1]), p1 = pack2(v0[2], v0[3]), p2 = pack2(v1[0], v1[1]), p3 = pack2(v1[2], v1[3]);
    v4u o = {wn ? p2 : p0, wn ? p3 : p1, wn ? p0 : p2, wn ? p1 : p3};
    *(GAS v4u*)(ob + jg * 1024) = o;
  }
  {
    f32x4 v = (acc8 - mean[0] * cs8) * rstd[0];
    v2u o = {pack2(v[0], v[1]), pack2(v[2], v[3])};
    *(GAS v2u*)(ob + 8 * 1024 + wn * 8) = o;
  }
  STAMP(0, 11);
  WAITV(0);
  STAMP(0, 12); STAMP_FLUSH(0);
}

__global__ __launch_bounds__(512, 2) void k2(Params p) {
  extern __shared__ __attribute__((aligned(16))) char smem[];
  LAS char* L = (LAS char*)smem;
  const int b = blockIdx.x, tid = threadIdx.x, l = tid & 63, w = __builtin_amdgcn_readfirstlane(tid >> 6);
  const int xcd = b & 7, jj = b >> 3, q = xcd >> 1, m = (xcd & 1) * 8 + (jj >> 2), n = jj & 3;
  STAMP_INIT(); STAMP(1, 0); STAMPC(1, 30);
  if (w >= 4) {
    const int lw = w - 4;
    if (lw < 2) {
      rsrc_t rs = mkrs(p.ws + WS_BF + (size_t)(q * 4 + n) * 16 * BSLOT, 16 * BSLOT);
      auto issue = [&](int st) {
        LAS char* dst = L + L_BR + (st & 3) * BSLOT + lw * 9216;
#pragma unroll
        for (int k = 0; k < 9; ++k) dma<0>(rs, dst + k * 1024, l * 16, st * BSLOT + lw * 9216 + k * 1024);
      };
      issue(0); issue(1); issue(2); issue(3);
      WAITV(27); BAR();
      for (int j = 1; j < 16; ++j) {
        if (j >= 2 && j + 2 < 16) issue(j + 2);
        if (j <= 13) WAITV(18); else if (j == 14) WAITV(9); else WAITV(0);
        BAR();
      }
    } else {
      rsrc_t rs = mkrs((q >> 1) ? p.Ci : p.Cr, S * S * 2);
      const int v0 = (l >> 3) * 4096 + ((l & 7) ^ (l >> 4)) * 16, v1 = (l >> 3) * 4096 + ((l & 7) ^ (4 + (l >> 4))) * 16;
      const int sb = (m * 128 + (lw - 2) * 64) * 4096 + (q & 1) * 2048;
      int slot = 0;
      auto issue = [&](int st) {
        LAS char* dst = L + L_AR + slot * ASLOT + (lw - 2) * 8192;
#pragma unroll
        for (int k = 0; k < 8; ++k) dma<0>(rs, dst + k * 1024, (k & 1) ? v1 : v0, sb + k * 8 * 4096 + st * 128);
        slot = slot == NAS - 1 ? 0 : slot + 1;
      };
      issue(0); issue(1); issue(2); issue(3); issue(4);
      WAITV(32); BAR();
      for (int j = 1; j < 16; ++j) {
        if (j >= 2 && j + 3 < 16) issue(j + 3);
        if (j <= 12) WAITV(24); else if (j == 13) WAITV(16); else if (j == 14) WAITV(8); else WAITV(0);
        BAR();
      }
    }
    STAMP_FLUSH(1);
    return;
  }
  const int wm = w >> 1, wn = w & 1;
  const int o0 = (l & 15) * 128 + (((l >> 4) ^ ((l & 15) >> 1)) * 16) + wm * 8192, o1 = o0 ^ 64;
  const int ob = l * 16 + wn * 4096, ob8 = l * 16 + 8192;
  f32x4 acc[4][4], acc8[2];
#pragma unroll
  for (int i = 0; i < 4; ++i)
#pragma unroll
    for (int j = 0; j < 4; ++j) acc[i][j] = f32x4{0, 0, 0, 0};
  acc8[0] = acc8[1] = f32x4{0, 0, 0, 0};
  v4u c0[4], a0[5], c1[4], a1[5];
  auto rd = [&](v4u (&c)[4], v4u (&a)[5], LAS char* ab, LAS char* bb, int o) {
#pragma unroll
    for (int i = 0; i < 4; ++i) { a[i] = LDSV4(bb + ob + i * 1024); c[i] = LDSV4(ab + o + (i ^ (2 * wn)) * 2048); }
    a[4] = LDSV4(bb + ob8);
  };
  auto mm = [&](v4u (&c)[4], v4u (&a)[5]) {
#pragma unroll
    for (int i = 0; i < 4; ++i)
#pragma unroll
      for (int j = 0; j < 4; ++j) acc[i][j] = mfma(a[j], c[i], acc[i][j]);
    acc8[0] = mfma(a[4], c[0], acc8[0]);
    acc8[1] = mfma(a[4], c[1], acc8[1]);
  };
#define ILV() do { _Pragma("unroll") for (int z = 0; z < 9; ++z) { __builtin_amdgcn_sched_group_barrier(0x008, 1, 0); __builtin_amdgcn_sched_group_barrier(0x100, 1, 0); } __builtin_amdgcn_sched_group_barrier(0x008, 9, 0); } while (0)
  int aslot = 0;
  BAR();
  STAMP(1, 1); STAMPC(1, 28);
  rd(c0, a0, L + L_AR, L + L_BR, o0);
  rd(c1, a1, L + L_AR, L + L_BR + 9216, o1);
  mm(c0, a0);
  WAITL0();
  for (int st = 1; st < 16; ++st) {
    aslot = aslot == NAS - 1 ? 0 : aslot + 1;
    LAS char* ab = L + L_AR + aslot * ASLOT;
    LAS char* bb = L + L_BR + (st & 3) * BSLOT;
    BAR();
    rd(c0, a0, ab, bb, o0);
    mm(c1, a1);
    ILV();
    __builtin_amdgcn_sched_barrier(0);
    rd(c1, a1, ab, bb + 9216, o1);
    mm(c0, a0);
    ILV();
    __builtin_amdgcn_sched_barrier(0);
    WAITL0();
  }
  mm(c1, a1);
  STAMP(1, 17); STAMPC(1, 29);
  const int t = l & 15, quad = l >> 4;
#pragma unroll
  for (int i = 0; i < 4; ++i) {
    const int rg = m * 16 + (wm * 4 + (i ^ (2 * wn))) * 2 + (t >> 3);
    GAS char* base = (GAS char*)p.ws + WS_PART + (size_t)((rg * 4 + q) * 4 + n) * 2304 + quad * 128 + (t & 7) * 16;
#pragma unroll
    for (int u = 0; u < 2; ++u) {
      const f32x4 x0 = acc[i][2 * u], x1 = acc[i][2 * u + 1];
      v4u o = {pack2(x0[0], x0[1]), pack2(x0[2], x0[3]), pack2(x1[0], x1[1]), pack2(x1[2], x1[3])};
      *(GAS v4u*)(base + (wn * 2 + u) * 512) = o;
    }
  }
#pragma unroll
  for (int i = 0; i < 2; ++i) {
    const int rg = m * 16 + (wm * 4 + wn * 2 + i) * 2 + (t >> 3);
    GAS char* base = (GAS char*)p.ws + WS_PART + (size_t)((rg * 4 + q) * 4 + n) * 2304 + 2048 + quad * 64 + (t & 7) * 8;
    v2u o = {pack2(acc8[i][0], acc8[i][1]), pack2(acc8[i][2], acc8[i][3])};
    *(GAS v2u*)base = o;
  }
  STAMP(1, 18);
  WAITV(0);
  STAMP(1, 19); STAMPC(1, 31); STAMP_FLUSH(1);
}

__device__ __forceinline__ unsigned fkey(float r) {
  unsigned u = __builtin_bit_cast(unsigned, r);
  return (u & 0x80000000u) ? ~u : (u | 0x80000000u);
}
__device__ __forceinline__ float unkey(unsigned k) { return __builtin_bit_cast(float, (k & 0x80000000u) ? (k & 0x7fffffffu) : ~k); }
constexpr unsigned SPIN_CAP = 1u << 20;

__device__ __forceinline__ u64 sweep_argmax(rsrc_t rsw, int l) {
  v4u g0, g1;
  for (unsigned sp = 0; sp < SPIN_CAP; ++sp) {
    g0 = __builtin_amdgcn_raw_buffer_load_b128(rsw, (int)WS_GRAN + l * 16, 0, AUX_SC1);
    g1 = __builtin_amdgcn_raw_buffer_load_b128(rsw, (int)WS_GRAN + 1024 + l * 16, 0, AUX_SC1);
    const bool ok = (g0[1] & g0[3] & g1[1] & g1[3] & 0x80000000u) != 0;
    if (__all(ok)) break;
    __builtin_amdgcn_s_sleep(1);
  }
  u64 k0 = ((u64)g0[0] << 32) | (unsigned)(2047 - (g0[1] & 2047)), k1 = ((u64)g0[2] << 32) | (unsigned)(2047 - (g0[3] & 2047));
  u64 k2 = ((u64)g1[0] << 32) | (unsigned)(2047 - (g1[1] & 2047)), k3 = ((u64)g1[2] << 32) | (unsigned)(2047 - (g1[3] & 2047));
  k0 = k0 > k1 ? k0 : k1; k2 = k2 > k3 ? k2 : k3; k0 = k0 > k2 ? k0 : k2;
#pragma unroll
  for (int o = 1; o < 64; o <<= 1) { const u64 t = __shfl_xor(k0, o); k0 = k0 > t ? k0 : t; }
  return k0;
}

__global__ __launch_bounds__(512, 2) void k3(Params p) {
  extern __shared__ __attribute__((aligned(16))) char smem[];
  LAS char* L = (LAS char*)smem;
  const int b = blockIdx.x, tid = threadIdx.x, l = tid & 63, w = __builtin_amdgcn_readfirstlane(tid >> 6);
  const int xcd = b & 7, jj = b >> 3;
  const int rg = b;
  rsrc_t rsw = mkrs(p.ws, (unsigned)WS_END);
  rsrc_t rso = mkrs(p.out, S * D * 2);
  const bool is_ffn = jj < 18;
  const int fb = xcd * 18 + jj;
  const bool is_red = jj >= 18 && (jj - 18) * 8 + xcd < 36;
  const int rb = (jj - 18) * 8 + xcd;
  LAS float* sst = (LAS float*)(L);
  LAS u64* swin = (LAS u64*)(L + 1024);
  LAS float* sgu = (LAS float*)(L + 2048);
  LAS float* sy = (LAS float*)(L + 4096);
  v4u wg[9], wu[9], wd[9];
  STAMP_INIT(); STAMP(2, 0);
  if (w >= 4) {
    if (is_ffn) {
      const int lw = w - 4;
      rsrc_t rg_ = mkrs(p.wg, D * F * 2), ru_ = mkrs(p.wu, D * F * 2), rd_ = mkrs(p.wd, D * F * 2);
      const int vo = (l >> 2) * 8 * (F * 2) + (l & 3) * 16, vo8 = (l >> 2) * (F * 2) + (l & 3) * 16;
#pragma unroll
      for (int k = 0; k < 8; ++k) {
        wg[k] = __builtin_amdgcn_raw_buffer_load_b128(rg_, vo, (144 * lw + k) * (F * 2) + fb * 64, 0);
        wu[k] = __builtin_amdgcn_raw_buffer_load_b128(ru_, vo, (144 * lw + k) * (F * 2) + fb * 64, 0);
      }
      wg[8] = __builtin_amdgcn_raw_buffer_load_b128(rg_, vo8, (144 * lw + 128) * (F * 2) + fb * 64, 0);
      wu[8] = __builtin_amdgcn_raw_buffer_load_b128(ru_, vo8, (144 * lw + 128) * (F * 2) + fb * 64, 0);
#pragma unroll
      for (int k = 0; k < 8; ++k) wd[k] = __builtin_amdgcn_raw_buffer_load_b128(rd_, l * 16, (fb * 32 + lw * 8 + k) * 1152, 0);
      wd[8] = __builtin_amdgcn_raw_buffer_load_b128(rd_, (l >> 3) * 1152 + 1024 + (l & 7) * 16, (fb * 32 + lw * 8) * 1152, 0);
      __builtin_amdgcn_sched_barrier(0);
    }
    BAR(); BAR();
  } else {
    const int row = l & 7, quad = (l >> 3) & 3, half = l >> 5, n = w;
    const int grow = rg * 8 + row;
    const int pb = (int)WS_PART + rg * 16 * 2304 + n * 2304;
    v4u pa[4], pc[4]; unsigned pe[4];
#pragma unroll
    for (int qq = 0; qq < 4; ++qq) {
      pa[qq] = __builtin_amdgcn_raw_buffer_load_b128(rsw, l * 16, pb + qq * 4 * 2304, 0);
      pc[qq] = __builtin_amdgcn_raw_buffer_load_b128(rsw, l * 16 + 1024, pb + qq * 4 * 2304, 0);
      pe[qq] = __builtin_amdgcn_raw_buffer_load_b32(rsw, 2048 + quad * 64 + row * 8 + half * 4, pb + qq * 4 * 2304, 0);
    }
    const int ea = n * 144 + 32 * half + 8 * quad, ec = ea + 64, ee = n * 144 + 128 + 4 * quad + 2 * half;
    const v4u xa = *(const GAS v4u*)(p.x + grow * 1152 + ea * 2), xc = *(const GAS v4u*)(p.x + grow * 1152 + ec * 2);
    const unsigned xe = *(const GAS unsigned*)(p.x + grow * 1152 + ee * 2);
    const v4u wa = *(const GAS v4u*)(p.wr + ea * 2), wc = *(const GAS v4u*)(p.wr + ec * 2);
    const unsigned we = *(const GAS unsigned*)(p.wr + ee * 2);
    float h[18], wv[18];
#pragma unroll
    for (int i = 0; i < 4; ++i) {
      h[2 * i] = bf2f(xa[i]); h[2 * i + 1] = bfhi(xa[i]); h[8 + 2 * i] = bf2f(xc[i]); h[9 + 2 * i] = bfhi(xc[i]);
      wv[2 * i] = bf2f(wa[i]); wv[2 * i + 1] = bfhi(wa[i]); wv[8 + 2 * i] = bf2f(wc[i]); wv[9 + 2 * i] = bfhi(wc[i]);
    }
    h[16] = bf2f(xe); h[17] = bfhi(xe); wv[16] = bf2f(we); wv[17] = bfhi(we);
#pragma unroll
    for (int qq = 0; qq < 4; ++qq) {
#pragma unroll
      for (int i = 0; i < 4; ++i) {
        h[2 * i] += bf2f(pa[qq][i]); h[2 * i + 1] += bfhi(pa[qq][i]);
        h[8 + 2 * i] += bf2f(pc[qq][i]); h[9 + 2 * i] += bfhi(pc[qq][i]);
      }
      h[16] += bf2f(pe[qq]); h[17] += bfhi(pe[qq]);
    }
    STAMP(2, 1);
    float s1 = 0, sw = 0, dt = 0;
#pragma unroll
    for (int i = 0; i < 18; ++i) { s1 += h[i]; sw += wv[i]; dt += h[i] * wv[i]; }
#pragma unroll
    for (int o = 8; o < 64; o <<= 1) { s1 += __shfl_xor(s1, o); sw += __shfl_xor(sw, o); dt += __shfl_xor(dt, o); }
    const float ml = s1 * (1.f / 144);
    float m2 = 0;
#pragma unroll
    for (int i = 0; i < 18; ++i) { const float d = h[i] - ml; m2 += d * d; }
#pragma unroll
    for (int o = 8; o < 64; o <<= 1) m2 += __shfl_xor(m2, o);
    if (l < 8) *(LAS f32x4*)(sst + (n * 8 + row) * 4) = f32x4{s1, m2, dt, sw};
    WAITL0(); BAR();
    f32x4 st[4];
#pragma unroll
    for (int i = 0; i < 4; ++i) st[i] = *(LAS f32x4*)(sst + (i * 8 + row) * 4);
    const float mean = ((st[0][0] + st[1][0]) + (st[2][0] + st[3][0])) * (1.f / D);
    float M2 = 0;
#pragma unroll
    for (int i = 0; i < 4; ++i) { const float d = st[i][0] * (1.f / 144) - mean; M2 += st[i][1] + 144.f * d * d; }
    const float rstd = 1.0f / sqrtf(M2 * (1.f / D) + 1e-5f);
    const float r = rstd * (((st[0][2] + st[1][2]) + (st[2][2] + st[3][2])) - mean * ((st[0][3] + st[1][3]) + (st[2][3] + st[3][3])));
    u64 key = ((u64)fkey(r) << 32) | (unsigned)(7 - row);
#pragma unroll
    for (int o = 1; o < 8; o <<= 1) { const u64 t = __shfl_xor(key, o); key = key > t ? key : t; }
    const int brow = 7 - (int)(key & 7);
    unsigned hn[9], ov[9];
#pragma unroll
    for (int i = 0; i < 9; ++i) {
      const float a = (h[2 * i] - mean) * rstd, c = (h[2 * i + 1] - mean) * rstd;
      hn[i] = pack2(a, c);
      ov[i] = pack2(h[2 * i] + a, h[2 * i + 1] + c);
    }
    if (row == brow) {
      const int cb = (int)WS_CAND + b * 1152;
      __builtin_amdgcn_raw_buffer_store_b128(v4u{hn[0], hn[1], hn[2], hn[3]}, rsw, cb + ea * 2, 0, AUX_SC1);
      __builtin_amdgcn_raw_buffer_store_b128(v4u{hn[4], hn[5], hn[6], hn[7]}, rsw, cb + ec * 2, 0, AUX_SC1);
      __builtin_amdgcn_raw_buffer_store_b32(hn[8], rsw, cb + ee * 2, 0, AUX_SC1);
    }
    __builtin_amdgcn_raw_buffer_store_b128(v4u{ov[0], ov[1], ov[2], ov[3]}, rso, grow * 1152 + ea * 2, 0, AUX_SC1);
    __builtin_amdgcn_raw_buffer_store_b128(v4u{ov[4], ov[5], ov[6], ov[7]}, rso, grow * 1152 + ec * 2, 0, AUX_SC1);
    __builtin_amdgcn_raw_buffer_store_b32(ov[8], rso, grow * 1152 + ee * 2, 0, AUX_SC1);
    STAMP(2, 2);
    WAITV(0); BAR();
    STAMP(2, 3);
    if (tid == 0) {
      const u64 g = ((u64)(0x80000000u | (unsigned)(rg * 8 + brow)) << 32) | (unsigned)(key >> 32);
      __hip_atomic_store((GAS u64*)(p.ws + WS_GRAN) + b, g, __ATOMIC_RELAXED, __HIP_MEMORY_SCOPE_AGENT);
    }
  }
  if (!is_ffn && !is_red) { STAMP_FLUSH(2); return; }
  if (w == 0) {
    const u64 k = sweep_argmax(rsw, l);
    if (l == 0) *swin = k;
    WAITL0();
  }
  BAR();
  STAMP(2, 4);
  const u64 win = *swin;
  const int wrow = 2047 - (int)(win & 2047);
  if (is_ffn) {
    if (w >= 4) {
      const int lw = w - 4;
      const int cb = (int)WS_CAND + (wrow >> 3) * 1152 + lw * 288;
      const v4u xv = __builtin_amdgcn_raw_buffer_load_b128(rsw, cb + (l >> 2) * 16, 0, AUX_SC1);
      const unsigned x8 = __builtin_amdgcn_raw_buffer_load_b16(rsw, cb + 256 + (l >> 2) * 2, 0, AUX_SC1);
      asm volatile("" :: "v"(xv), "v"(x8));
      STAMP(2, 5);
      float g[8], u[8];
#pragma unroll
      for (int i = 0; i < 8; ++i) g[i] = u[i] = 0.f;
#pragma unroll
      for (int k = 0; k < 9; ++k) {
        unsigned xlo, xhi;
        if (k < 8) { const unsigned t = xv[k >> 1]; xlo = (k & 1) ? t >> 16 : t & 0xffffu; xhi = (k & 1) ? t & 0xffff0000u : t << 16; }
        else { xlo = x8 & 0xffffu; xhi = x8 << 16; }
#pragma unroll
        for (int i = 0; i < 4; ++i) {
          const unsigned a = wg[k][i], c = wu[k][i];
          g[2 * i] = dot2(a, xlo, g[2 * i]); g[2 * i + 1] = dot2(a, xhi, g[2 * i + 1]);
          u[2 * i] = dot2(c, xlo, u[2 * i]); u[2 * i + 1] = dot2(c, xhi, u[2 * i + 1]);
        }
      }
#pragma unroll
      for (int o = 4; o < 64; o <<= 1)
#pragma unroll
        for (int i = 0; i < 8; ++i) { g[i] += __shfl_xor(g[i], o); u[i] += __shfl_xor(u[i], o); }
      if (l < 4) {
        *(LAS f32x4*)(sgu + lw * 64 + l * 8) = f32x4{g[0], g[1], g[2], g[3]};
        *(LAS f32x4*)(sgu + lw * 64 + l * 8 + 4) = f32x4{g[4], g[5], g[6], g[7]};
        *(LAS f32x4*)(sgu + lw * 64 + 32 + l * 8) = f32x4{u[0], u[1], u[2], u[3]};
        *(LAS f32x4*)(sgu + lw * 64 + 32 + l * 8 + 4) = f32x4{u[4], u[5], u[6], u[7]};
      }
      WAITL0();
    }
    BAR();
    if (w >= 4) {
      const int lw = w - 4;
      STAMP(2, 6);
      const int f = 8 * lw + (l >> 3);
      const float gg = (sgu[f] + sgu[64 + f]) + (sgu[128 + f] + sgu[192 + f]);
      const float uu = (sgu[32 + f] + sgu[96 + f]) + (sgu[160 + f] + sgu[224 + f]);
      const float gu = gg / (1.f + __expf(-gg)) * uu;
      const unsigned gb = pack2(gu, gu);
      float y[8], z[8];
#pragma unroll
      for (int i = 0; i < 8; ++i) y[i] = 0.f;
#pragma unroll
      for (int k = 0; k < 8; ++k) {
        const unsigned sv = __builtin_amdgcn_readlane(gb, k * 8);
        const unsigned lo = sv & 0xffffu, hi = sv & 0xffff0000u;
#pragma unroll
        for (int i = 0; i < 4; ++i) { const unsigned a = wd[k][i]; y[2 * i] = dot2(a, lo, y[2 * i]); y[2 * i + 1] = dot2(a, hi, y[2 * i + 1]); }
      }
      const float gf = bf2f(gb & 0xffffu);
#pragma unroll
      for (int i = 0; i < 4; ++i) { const unsigned a = wd[8][i]; z[2 * i] = bf2f(a & 0xffffu) * gf; z[2 * i + 1] = bfhi(a) * gf; }
#pragma unroll
      for (int o = 8; o < 64; o <<= 1)
#pragma unroll
        for (int i = 0; i < 8; ++i) z[i] += __shfl_xor(z[i], o);
      *(LAS f32x4*)(sy + lw * 576 + l * 8) = f32x4{y[0], y[1], y[2], y[3]};
      *(LAS f32x4*)(sy + lw * 576 + l * 8 + 4) = f32x4{y[4], y[5], y[6], y[7]};
      if (l < 8) {
        *(LAS f32x4*)(sy + lw * 576 + 512 + l * 8) = f32x4{z[0], z[1], z[2], z[3]};
        *(LAS f32x4*)(sy + lw * 576 + 512 + l * 8 + 4) = f32x4{z[4], z[5], z[6], z[7]};
      }
      WAITL0();
    }
    BAR();
    STAMP(2, 7);
    for (int e = tid; e < D; e += 512) {
      const float v = (sy[e] + sy[576 + e]) + (sy[1152 + e] + sy[1728 + e]);
      const u64 g = (1ull << 32) | __builtin_bit_cast(unsigned, v);
      __hip_atomic_store((GAS u64*)(p.ws + WS_YG) + ((e >> 4) * 144 + fb) * 16 + (e & 15), g, __ATOMIC_RELAXED, __HIP_MEMORY_SCOPE_AGENT);
    }
  } else {
    float a0 = 0, a1 = 0;
    unsigned base_out = 0;
    if (tid < 8) base_out = __builtin_amdgcn_raw_buffer_load_b32(rso, wrow * 1152 + rb * 32 + tid * 4, 0, AUX_SC1);
    if (w < 6) {
      v4u g[3];
      for (unsigned sp = 0; sp < SPIN_CAP; ++sp) {
#pragma unroll
        for (int k = 0; k < 3; ++k) g[k] = __builtin_amdgcn_raw_buffer_load_b128(rsw, (int)WS_YG + rb * 18432 + (w * 3 + k) * 1024 + l * 16, 0, AUX_SC1);
        const bool ok = (g[0][1] & g[0][3] & g[1][1] & g[1][3] & g[2][1] & g[2][3]) == 1u;
        if (__all(ok)) break;
        __builtin_amdgcn_s_sleep(1);
      }
#pragma unroll
      for (int k = 0; k < 3; ++k) { const unsigned t0 = g[k][0], t1 = g[k][2]; a0 += __builtin_bit_cast(float, t0); a1 += __builtin_bit_cast(float, t1); }
#pragma unroll
      for (int o = 8; o < 64; o <<= 1) { a0 += __shfl_xor(a0, o); a1 += __shfl_xor(a1, o); }
      if (l < 8) { sy[w * 16 + 2 * l] = a0; sy[w * 16 + 2 * l + 1] = a1; }
      WAITL0();
    }
    BAR();
    STAMP(2, 8);
    if (tid < 8) {
      float y0 = 0, y1 = 0;
#pragma unroll
      for (int k = 0; k < 6; ++k) { y0 += sy[k * 16 + 2 * tid]; y1 += sy[k * 16 + 2 * tid + 1]; }
      const float r = unkey((unsigned)(win >> 32));
      const float sg = 1.f / (1.f + __expf(-r));
      const unsigned o = pack2(bf2f(base_out & 0xffffu) + y0 * sg, bfhi(base_out) + y1 * sg);
      __builtin_amdgcn_raw_buffer_store_b32(o, rso, wrow * 1152 + rb * 32 + tid * 4, 0, AUX_SC1);
    }
  }
  STAMP(2, 9);
  STAMP_FLUSH(2);
}

extern "C" void kernel_launch(void* const* d_in, const int* in_sizes, int n_in, void* d_out, int out_size, void* d_ws,
                              size_t ws_size, hipStream_t stream) {
  Params p;
  p.x = (const char*)d_in[0]; p.Cr = (const char*)d_in[1]; p.Ci = (const char*)d_in[2];
  p.Dr = (const char*)d_in[3]; p.Di = (const char*)d_in[4]; p.wr = (const char*)d_in[6];
  p.wg = (const char*)d_in[7]; p.wu = (const char*)d_in[8]; p.wd = (const char*)d_in[9];
  p.out = (char*)d_out; p.ws = (char*)d_ws;
  hipFuncSetAttribute((const void*)k1, hipFuncAttributeMaxDynamicSharedMemorySize, LDS_BYTES);
  hipFuncSetAttribute((const void*)k2, hipFuncAttributeMaxDynamicSharedMemorySize, LDS_BYTES);
  hipFuncSetAttribute((const void*)k3, hipFuncAttributeMaxDynamicSharedMemorySize, LDS_BYTES);
  k1<<<256, 512, LDS_BYTES, stream>>>(p);
  k2<<<256, 512, LDS_BYTES, stream>>>(p);
  k3<<<256, 512, LDS_BYTES, stream>>>(p);
}
```

```cpp
#include <hip/hip_runtime.h>
#include <hip/hip_bf16.h>
#include <stdint.h>

#define LAS __attribute__((address_space(3)))
#define GAS __attribute__((address_space(1)))
#define CAS __attribute__((address_space(4)))
typedef short bf16x8 __attribute__((ext_vector_type(8)));
typedef __bf16 bf16x2 __attribute__((ext_vector_type(2)));
typedef float f32x4 __attribute__((ext_vector_type(4)));
typedef float f32x2 __attribute__((ext_vector_type(2)));
typedef unsigned v4u __attribute__((ext_vector_type(4)));
typedef unsigned v2u __attribute__((ext_vector_type(2)));
typedef unsigned long long u64;
typedef __amdgpu_buffer_rsrc_t rsrc_t;

constexpr int S = 2048, D = 576, F = 4608;
constexpr int AUX_SC1 = 16;
#define NPOST 0
#define PSLEEP 2
#define AUX_XD 0
#define AUX_C 0
#define AUX_W 0
constexpr int WS_EPOCH = 0;
constexpr int WS_F3 = 4096;
constexpr int WS_GRAN = 8192;
constexpr int WS_F2 = 32768;
constexpr int WS_F1 = 65536;
constexpr int WS_CAND = 131072;
constexpr int WS_YG = WS_CAND + 128 * 2304;
constexpr int WS_BF = 2 << 20;
constexpr int WS_PART = WS_BF + 256 * 18432;
constexpr int WS_END = WS_PART + 256 * 16 * 2304;
constexpr int L_BR = 0, BSLOT = 18432;
constexpr int L_AR = 4 * BSLOT, ASLOT = 16384;
constexpr int L_MISC = L_AR + 9 * 8192;
constexpr int L_DBG = L_MISC + 2048;
constexpr int L_WR = L_MISC + 4096;
constexpr int L_SC = L_WR + 2048;
constexpr int LDS_BYTES = L_SC + 8192;
constexpr int CODE_LINES = 180;
constexpr unsigned SPIN_CAP = 1u << 20;

struct Params {
  const char *x, *Cr, *Ci, *Dr, *Di, *wr, *wg, *wu, *wd;
  char* out;
  char* ws;
};

__device__ __forceinline__ float asf(unsigned u) { return __builtin_bit_cast(float, u); }
__device__ __forceinline__ unsigned asu(float f) { return __builtin_bit_cast(unsigned, f); }
__device__ __forceinline__ float bflo(unsigned b) { return asf(b << 16); }
__device__ __forceinline__ float bfhi(unsigned b) { return asf(b & 0xffff0000u); }
__device__ __forceinline__ unsigned pack2(float lo, float hi) {
  bf16x2 v; v.x = (__bf16)lo; v.y = (__bf16)hi;
  return __builtin_bit_cast(unsigned, v);
}
__device__ __forceinline__ f32x2 unp(unsigned v) { return f32x2{bflo(v), bfhi(v)}; }
__device__ __forceinline__ unsigned pk(f32x2 v) { return pack2(v[0], v[1]); }
__device__ __forceinline__ float dot2(unsigned a, unsigned b, float c) {
  return __builtin_amdgcn_fdot2_f32_bf16(__builtin_bit_cast(bf16x2, a), __builtin_bit_cast(bf16x2, b), c, false);
}
__device__ __forceinline__ f32x4 mfma(v4u a, v4u b, f32x4 c) {
  return __builtin_amdgcn_mfma_f32_16x16x32_bf16(__builtin_bit_cast(bf16x8, a), __builtin_bit_cast(bf16x8, b), c, 0, 0, 0);
}
template <int CTRL> __device__ __forceinline__ unsigned dppu(unsigned v) { return (unsigned)__builtin_amdgcn_update_dpp(0, (int)v, CTRL, 0xf, 0xf, true); }
template <int CTRL> __device__ __forceinline__ float dppf(float v) { return asf(dppu<CTRL>(asu(v))); }
constexpr int DPP_X1 = 0xB1, DPP_X2 = 0x4E, DPP_HM = 0x141, DPP_R4 = 0x124, DPP_R8 = 0x128;
__device__ __forceinline__ float red16(float a, float b) { auto r = __builtin_amdgcn_permlane16_swap(asu(a), asu(b), false, false); return asf(r[0]) + asf(r[1]); }
__device__ __forceinline__ float red32(float a, float b) { auto r = __builtin_amdgcn_permlane32_swap(asu(a), asu(b), false, false); return asf(r[0]) + asf(r[1]); }
__device__ __forceinline__ rsrc_t mkrs(const void* p, unsigned bytes) { return __builtin_amdgcn_make_buffer_rsrc((void*)p, 0, bytes, 0x00020000); }
template <int AUX> __device__ __forceinline__ void dma(rsrc_t rs, LAS char* dst, int voff, int soff) {
  __builtin_amdgcn_raw_ptr_buffer_load_lds(rs, dst, 16, voff, soff, 0, AUX);
}
#define BAR() asm volatile("s_barrier" ::: "memory")
#define WAITV(n) asm volatile("s_waitcnt vmcnt(%0)" ::"n"(n) : "memory")
#define WAITL0() asm volatile("s_waitcnt lgkmcnt(0)" ::: "memory")
#define SCHED() __builtin_amdgcn_sched_barrier(0)
#define LDSV4(p) (*(LAS v4u*)(p))
#define SGB(m, n) __builtin_amdgcn_sched_group_barrier(m, n, 0)

#define STAMP(i)
#define STAMP_INIT()
#define STAMP_FLUSH()

__device__ __forceinline__ unsigned fkey(float r) { const unsigned u = asu(r); return (u & 0x80000000u) ? ~u : (u | 0x80000000u); }
__device__ __forceinline__ float unkey(unsigned k) { return asf((k & 0x80000000u) ? (k & 0x7fffffffu) : ~k); }
__device__ __forceinline__ unsigned umax(unsigned a, unsigned b) { return a > b ? a : b; }

__global__ __launch_bounds__(512, 2) void fused(Params p) {
  extern __shared__ __attribute__((aligned(16))) char smem[];
  LAS char* L = (LAS char*)smem;
  u64 pc0;
  asm volatile("s_getpc_b64 %0" : "=s"(pc0));
  const int b = blockIdx.x, tid = threadIdx.x, l = tid & 63, w = __builtin_amdgcn_readfirstlane(tid >> 6);
  const int xcd = b & 7, jj = b >> 3, q = xcd >> 1, mh = xcd & 1, mp = jj >> 2, n = jj & 3, m = mh * 8 + mp;
  const int sig = (2 * mp + mh + 4 * n) & 15, panel = q * 4 + n;
  const rsrc_t rsw = mkrs(p.ws, WS_END);
  u64 E;
#define LOAD_EPOCH() do { asm volatile("s_load_dwordx2 %0, %1, 0x0\n s_waitcnt lgkmcnt(0)" : "=s"(E) : "s"(p.ws) : "memory"); E += 1ull; } while (0)
  const bool is_f = jj < 9, is_p = jj >= 9 && jj < 25;
  const int fb = xcd * 9 + jj, pi = xcd * 16 + jj - 9, rb = (jj - 25) * 8 + xcd;
  asm volatile("" :: "s"(p.x), "s"(p.Dr), "s"(p.ws));
  v4u wg[9], wu[9], wd[9];
  STAMP_INIT(); STAMP(0);
#define LOADW_GU(dst, ptr) do { const rsrc_t r_ = mkrs(ptr, D * F * 2); \
    _Pragma("unroll") for (int k = 0; k < 8; ++k) dst[k] = __builtin_amdgcn_raw_buffer_load_b128(r_, (l >> 3) * 8 * (F * 2) + (l & 7) * 16, (144 * (w >> 1) + 64 * (w & 1) + k) * (F * 2) + fb * 128, AUX_W); \
    dst[8] = __builtin_amdgcn_raw_buffer_load_b128(r_, (l >> 3) * (F * 2) + (l & 7) * 16, (144 * (w >> 1) + 128 + 8 * (w & 1)) * (F * 2) + fb * 128, AUX_W); } while (0)
#define LOADW_G() LOADW_GU(wg, p.wg)
#define LOADW_U() LOADW_GU(wu, p.wu)
#define LOADW_D() do { const rsrc_t r_ = mkrs(p.wd, D * F * 2); \
    _Pragma("unroll") for (int k = 0; k < 8; ++k) wd[k] = __builtin_amdgcn_raw_buffer_load_b128(r_, l * 16, (fb * 64 + w * 8 + k) * 1152, AUX_W); \
    wd[8] = __builtin_amdgcn_raw_buffer_load_b128(r_, (l >> 3) * 1152 + 1024 + (l & 7) * 16, (fb * 64 + w * 8) * 1152, AUX_W); } while (0)

  if (__builtin_expect(w >= 4, 1)) {
    const int lw = w & 1;
    const int rperm = 8 * (l >> 5) + ((l >> 3) & 3), rlin = l >> 3;
    const int c0 = ((l & 7) ^ (l >> 4)) * 16, c1 = c0 ^ 64;
    int ko = mp * 128;
    if (w < 6) {
      {
        const rsrc_t rsd = mkrs((q >> 1) ? p.Di : p.Dr, D * D * 2);
        int vk[9], sk[9];
#pragma unroll
        for (int k = 0; k < 9; ++k) {
          const int P = lw * 9 + k, j = P >> 1, odd = P & 1;
          const int sc = j < 8 ? 32 * (j >> 1) + 4 * (j & 1) + 16 * odd : 128 + 8 * odd;
          sk[k] = (n * 144 + sc) * 1152;
          vk[k] = (j < 8 ? rperm : rlin) * 1152 + (odd ? c1 : c0);
        }
#define ISSUED(st) do { \
          _Pragma("unroll") for (int k = 0; k < 9; ++k) dma<AUX_XD>(rsd, L + L_BR + ((st) & 3) * BSLOT + lw * 9216 + k * 1024, vk[k], sk[k] + ko); \
          ko = ko == 1024 ? 0 : ko + 128; } while (0)
        STAMP(18);
        ISSUED(0); ISSUED(1);
        LOAD_EPOCH();
        WAITV(9); STAMP(21); BAR();
        ISSUED(2); ISSUED(3); WAITV(18); BAR();
#pragma unroll 1
        for (int j = 2; j < 7; ++j) { ISSUED(j + 2); WAITV(18); BAR(); }
        WAITV(9); BAR();
        WAITV(0); STAMP(31); BAR();
      }
      BAR();
      BAR();
      STAMP(1);
      const int pbase = WS_BF + panel * 16 * BSLOT + lw * 9216;
      GAS u64* f1 = (GAS u64*)(p.ws + WS_F1) + (panel * 2 + lw) * 256 + ((l & 31) >> 1) * 16 + (l & 1);
      unsigned have = 0;
      __builtin_amdgcn_s_sleep(20);
#define NEED(st) do { const unsigned bit_ = 3u << (2 * ((sig + (st)) & 15)); \
        for (unsigned sp_ = 0; (have & bit_) != bit_ && sp_ < SPIN_CAP; ++sp_) { \
          const u64 v_ = __hip_atomic_load(f1, __ATOMIC_RELAXED, __HIP_MEMORY_SCOPE_AGENT); \
          have = (unsigned)__builtin_amdgcn_ballot_w64(v_ == E); if ((have & bit_) != bit_) __builtin_amdgcn_s_sleep(PSLEEP); } } while (0)
#define ISSUEB(st) do { NEED(st); const int so_ = pbase + ((sig + (st)) & 15) * BSLOT; \
        _Pragma("unroll") for (int k = 0; k < 9; ++k) dma<AUX_SC1>(rsw, L + L_BR + ((st) & 3) * BSLOT + lw * 9216 + k * 1024, l * 16, so_ + k * 1024); } while (0)
      ISSUEB(1);
      STAMP(3);
      ISSUEB(2);
      WAITV(9); BAR();
      STAMP(4);
      ISSUEB(3);
#pragma unroll 1
      for (int j = 2; j < 14; ++j) { ISSUEB(j + 2); WAITV(18); BAR(); }
    } else {
      const rsrc_t rsx = mkrs(p.x, S * D * 2);
      {
        const int vx0 = rperm * 1152 + c0, vx1 = rperm * 1152 + c1, sx = (((q & 1) * 16 + sig) * 64 + 32 * lw) * 1152;
#define ISSUEX(st) do { \
          _Pragma("unroll") for (int k = 0; k < 4; ++k) dma<AUX_XD>(rsx, L + L_AR + (st) * 8192 + lw * 4096 + k * 1024, (k & 1) ? vx1 : vx0, sx + (4 * (k >> 1) + 16 * (k & 1)) * 1152 + ko); \
          ko = ko == 1024 ? 0 : ko + 128; } while (0)
        ISSUEX(0); ISSUEX(1); ISSUEX(2); ISSUEX(3);
        LOAD_EPOCH();
        WAITV(12); BAR();
        WAITV(8); BAR();
#pragma unroll 1
        for (int j = 2; j < 7; ++j) { ISSUEX(j + 2); WAITV(8); BAR(); }
      }
      const rsrc_t rsc = mkrs((q >> 1) ? p.Ci : p.Cr, S * S * 2);
      const int v0 = (l >> 3) * 4096 + c0, v1 = v0 ^ 64;
      const int sb = (m * 128 + lw * 64) * 4096 + (q & 1) * 2048;
#define ISSUEA(st) do { const int so_ = sb + ((sig + (st)) & 15) * 128; \
        _Pragma("unroll") for (int k = 0; k < 8; ++k) dma<AUX_C>(rsc, L + L_AR + ((st) & 3) * ASLOT + lw * 8192 + k * 1024, (k & 1) ? v1 : v0, so_ + k * 8 * 4096); } while (0)
      ISSUEA(0);
      WAITV(12); BAR();
      ISSUEA(1);
      WAITV(16); BAR();
      BAR();
      WAITV(8); BAR();
      WAITV(8 * NPOST); BAR();
      ISSUEA(2);
      ISSUEA(3);
#pragma unroll 1
      for (int j = 2; j < 14; ++j) { ISSUEA(j + 2); WAITV(16); BAR(); }
    }
    if (is_p) {
      if (w < 6) {
        const rsrc_t rsx = mkrs(p.x, S * D * 2);
#pragma unroll
        for (int k = 0; k < 9; ++k) dma<0>(rsx, L + L_BR + lw * 9216 + k * 1024, l * 16, pi * BSLOT + lw * 9216 + k * 1024);
        WAITV(18); BAR(); WAITV(9); BAR();
      } else {
        dma<0>(mkrs(p.wr, D * 2), L + L_WR + lw * 1024, l * 16, lw * 1024);
        WAITV(9); BAR(); WAITV(1); BAR();
      }
      WAITV(0);
      BAR();
    } else if (is_f) {
      LOADW_G(); SCHED();
      if (w < 6) WAITV(18); else WAITV(17);
      BAR(); WAITV(9); BAR();
      BAR();
      LOADW_U(); LOADW_D(); SCHED();
    } else {
      if (w < 6) WAITV(9); else WAITV(8);
      BAR(); WAITV(0); BAR();
    }
    STAMP(5);
  } else {
    if (tid < 81) __builtin_amdgcn_raw_buffer_store_b128(v4u{0, 0, 0, 0}, rsw, tid * 16, WS_YG + b * 1296, AUX_SC1);
    unsigned warm = 0;
    if (w == 3 && l < 8 && jj * 8 + l < CODE_LINES) warm = *(const GAS unsigned*)((pc0 & ~255ull) + (jj * 8 + l) * 128);
    LOAD_EPOCH();
    const int wa = w >> 1, wn = w & 1;
    const int o0 = (l & 15) * 128 + (((l >> 4) ^ ((l & 15) >> 1)) * 16), o1 = o0 ^ 64;
    {
      f32x4 acc[2][4], acc8 = {0, 0, 0, 0}, cs0 = {0, 0, 0, 0}, cs1 = {0, 0, 0, 0}, cs8 = {0, 0, 0, 0};
      const v4u ones = {0x3f803f80u, 0x3f803f80u, 0x3f803f80u, 0x3f803f80u};
#pragma unroll
      for (int t = 0; t < 2; ++t)
#pragma unroll
        for (int j = 0; j < 4; ++j) acc[t][j] = f32x4{0, 0, 0, 0};
      f32x4 sx = {0, 0, 0, 0}, gx = {0, 0, 0, 0};
      v4u x0[2], d0[5], x1[2], d1[5];
#define RD1(xf, df, st, o) do { \
        _Pragma("unroll") for (int t = 0; t < 2; ++t) xf[t] = LDSV4(L + L_AR + (st) * 8192 + (2 * wa + (t ^ wn)) * 2048 + (o)); \
        _Pragma("unroll") for (int j = 0; j < 4; ++j) df[j] = LDSV4(L + L_BR + ((st) & 3) * BSLOT + (4 * wn + (j ^ (2 * wa))) * 2048 + (o)); \
        df[4] = LDSV4(L + L_BR + ((st) & 3) * BSLOT + 8 * 2048 + (o)); } while (0)
#define MM1(xf, df) do { \
        cs0 = mfma(ones, df[0], cs0); cs1 = mfma(ones, df[1], cs1); cs8 = mfma(ones, df[4], cs8); sx = mfma(ones, xf[0], sx); gx = mfma(xf[0], xf[0], gx); \
        _Pragma("unroll") for (int t = 0; t < 2; ++t) _Pragma("unroll") for (int j = 0; j < 4; ++j) acc[t][j] = mfma(xf[t], df[j], acc[t][j]); \
        acc8 = mfma(xf[0], df[4], acc8); } while (0)
#define ILV1() do { _Pragma("unroll") for (int z = 0; z < 7; ++z) { SGB(0x008, 1); SGB(0x100, 1); } SGB(0x008, 7); SCHED(); } while (0)
      BAR();
      STAMP(1);
      RD1(x0, d0, 0, o0); RD1(x1, d1, 0, o1); MM1(x0, d0); SCHED(); WAITL0();
#pragma unroll
      for (int st = 1; st < 9; ++st) {
        BAR();
        RD1(x0, d0, st, o0); MM1(x1, d1); ILV1();
        RD1(x1, d1, st, o1); MM1(x0, d0); ILV1();
        WAITL0();
      }
      MM1(x1, d1);
      STAMP(2);
      LAS float* smean = (LAS float*)(L + L_MISC);
      LAS float* srstd = smean + 64;
      LAS float* scs = smean + 128;
      if (((l & 15) >> 2) == (l >> 4)) {
        const float s2 = (l & 2) ? ((l & 1) ? gx[3] : gx[2]) : ((l & 1) ? gx[1] : gx[0]);
        const float mean = sx[0] * (1.f / D);
        const float var = fmaxf(s2 * (1.f / D) - mean * mean, 0.f);
        const float rs = __builtin_amdgcn_rsqf(var + 1e-5f);
        smean[w * 16 + (l & 15)] = mean;
        srstd[w * 16 + (l & 15)] = (q >> 1) ? -rs : rs;
        scs[(4 * wn + 2 * wa) * 16 + (l & 15)] = cs0[0];
        scs[(4 * wn + 2 * wa + 1) * 16 + (l & 15)] = cs1[0];
      }
      WAITL0(); BAR();
      f32x4 mean[2], rstd[2];
#pragma unroll
      for (int t = 0; t < 2; ++t) {
        mean[t] = *(LAS f32x4*)(smean + (2 * wa + (t ^ wn)) * 16 + 4 * (l >> 4));
        rstd[t] = *(LAS f32x4*)(srstd + (2 * wa + (t ^ wn)) * 16 + 4 * (l >> 4));
      }
      LAS char* ob = L + L_BR + wa * 9216 + l * 16;
      const int gb = WS_BF + (panel * 16 + sig) * BSLOT + wa * 9216;
#pragma unroll
      for (int j = 0; j < 4; ++j) {
        const int jg = 4 * wn + (j ^ (2 * wa));
        const float c = j == 0 ? cs0[0] : j == 1 ? cs1[0] : scs[jg * 16 + (l & 15)];
        const f32x4 v0 = (acc[0][j] - mean[0] * c) * rstd[0], v1 = (acc[1][j] - mean[1] * c) * rstd[1];
        const unsigned p0 = pack2(v0[0], v0[1]), p1 = pack2(v0[2], v0[3]), p2 = pack2(v1[0], v1[1]), p3 = pack2(v1[2], v1[3]);
        const v4u o = {wn ? p2 : p0, wn ? p3 : p1, wn ? p0 : p2, wn ? p1 : p3};
        __builtin_amdgcn_raw_buffer_store_b128(o, rsw, l * 16, gb + jg * 1024, AUX_SC1);
        LDSV4(ob + jg * 1024) = o;
      }
      {
        const f32x4 v = (acc8 - mean[0] * cs8[0]) * rstd[0];
        const v2u o = {pack2(v[0], v[1]), pack2(v[2], v[3])};
        __builtin_amdgcn_raw_buffer_store_b64(o, rsw, l * 16, gb + 8 * 1024 + wn * 8, AUX_SC1);
        *(LAS v2u*)(ob + 8 * 1024 + wn * 8) = o;
      }
      WAITL0(); BAR();
    }
    STAMP(3);
    const int oa0 = o0 + wa * 8192, oa1 = o1 + wa * 8192, ob = l * 16 + wn * 4096, ob8 = l * 16 + 8192;
    f32x4 acc[4][4], acc8[2];
#pragma unroll
    for (int i = 0; i < 4; ++i)
#pragma unroll
      for (int j = 0; j < 4; ++j) acc[i][j] = f32x4{0, 0, 0, 0};
    acc8[0] = acc8[1] = f32x4{0, 0, 0, 0};
    v4u c0[4], a0[5], c1[4], a1[5];
#define RD2(c, a, slot, ks, o) do { \
      _Pragma("unroll") for (int i = 0; i < 4; ++i) { a[i] = LDSV4(L + L_BR + (slot) * BSLOT + (ks) * 9216 + ob + i * 1024); c[i] = LDSV4(L + L_AR + (slot) * ASLOT + (o) + (i ^ (2 * wn)) * 2048); } \
      a[4] = LDSV4(L + L_BR + (slot) * BSLOT + (ks) * 9216 + ob8); } while (0)
#define MM2(c, a) do { \
      _Pragma("unroll") for (int i = 0; i < 4; ++i) _Pragma("unroll") for (int j = 0; j < 4; ++j) acc[i][j] = mfma(a[j], c[i], acc[i][j]); \
      acc8[0] = mfma(a[4], c[0], acc8[0]); acc8[1] = mfma(a[4], c[1], acc8[1]); } while (0)
#define ILV2() do { _Pragma("unroll") for (int z = 0; z < 9; ++z) { SGB(0x008, 1); SGB(0x100, 1); } SGB(0x008, 9); SCHED(); } while (0)
#define STEP2(slot) do { BAR(); RD2(c0, a0, slot, 0, oa0); MM2(c1, a1); ILV2(); RD2(c1, a1, slot, 1, oa1); MM2(c0, a0); ILV2(); WAITL0(); } while (0)
    RD2(c0, a0, 0, 0, oa0); RD2(c1, a1, 0, 1, oa1); MM2(c0, a0); SCHED(); WAITL0();
    WAITV(0);
    asm volatile("" :: "v"(warm));
    if (l == 0) __hip_atomic_store((GAS u64*)(p.ws + WS_F1) + ((panel * 2 + wa) * 16 + sig) * 16 + wn, E, __ATOMIC_RELAXED, __HIP_MEMORY_SCOPE_AGENT);
    STAMP(19);
    STEP2(1);
    STAMP(4);
    STEP2(2); STEP2(3);
#pragma unroll 1
    for (int it = 0; it < 3; ++it) { STEP2(0); STEP2(1); STEP2(2); STEP2(3); }
    MM2(c1, a1);
    STAMP(5);
    const int t = l & 15, quad = l >> 4;
    const int pv = (t >> 3) * (16 * 2304) + quad * 128 + (t & 7) * 16, pv8 = (t >> 3) * (16 * 2304) + 2048 + quad * 64 + (t & 7) * 8;
#pragma unroll
    for (int i = 0; i < 4; ++i) {
      const int so = WS_PART + ((m * 16 + (wa * 4 + (i ^ (2 * wn))) * 2) * 16 + panel) * 2304;
#pragma unroll
      for (int u = 0; u < 2; ++u) {
        const f32x4 y0 = acc[i][2 * u], y1 = acc[i][2 * u + 1];
        __builtin_amdgcn_raw_buffer_store_b128(v4u{pack2(y0[0], y0[1]), pack2(y0[2], y0[3]), pack2(y1[0], y1[1]), pack2(y1[2], y1[3])}, rsw, pv, so + (wn * 2 + u) * 512, AUX_SC1);
      }
      if (i < 2) __builtin_amdgcn_raw_buffer_store_b64(v2u{pack2(acc8[i][0], acc8[i][1]), pack2(acc8[i][2], acc8[i][3])}, rsw, pv8, so, AUX_SC1);
    }
    STAMP(6);
    if (is_p) BAR();
    WAITV(0);
    STAMP(7);
    if (l == 0) __hip_atomic_store((GAS u64*)(p.ws + WS_F2) + (m * 16 + panel) * 16 + w, E, __ATOMIC_RELAXED, __HIP_MEMORY_SCOPE_AGENT);
    SCHED();
    if (is_f) { BAR(); LOADW_G(); LOADW_U(); LOADW_D(); SCHED(); }
    STAMP(20);
  }
  const unsigned tag = (unsigned)E;
  const rsrc_t rso = mkrs(p.out, S * D * 2);
  if (is_p) {
    LAS float* sst = (LAS float*)(L + L_SC);
    const int row = l & 7, quad = (l >> 3) & 3, half = l >> 5, nn = w & 3, rh = w >> 2;
    const int ea = (nn * 144 + 32 * half + 8 * quad) * 2, ee = (nn * 144 + 128 + 4 * quad + 2 * half) * 2, ro = (rh * 8 + row) * 1152;
    f32x2 h[9], wv[9];
    {
      const v4u xa = LDSV4(L + L_BR + ro + ea), xc = LDSV4(L + L_BR + ro + ea + 128), wa4 = LDSV4(L + L_WR + ea), wc4 = LDSV4(L + L_WR + ea + 128);
      const unsigned xe = *(LAS unsigned*)(L + L_BR + ro + ee), we = *(LAS unsigned*)(L + L_WR + ee);
#pragma unroll
      for (int i = 0; i < 4; ++i) { h[i] = unp(xa[i]); h[4 + i] = unp(xc[i]); wv[i] = unp(wa4[i]); wv[4 + i] = unp(wc4[i]); }
      h[8] = unp(xe); wv[8] = unp(we);
    }
    float sw;
    { const f32x2 t = ((wv[0] + wv[1]) + (wv[2] + wv[3])) + ((wv[4] + wv[5]) + (wv[6] + wv[7])) + wv[8]; sw = t[0] + t[1]; }
    sw += dppf<DPP_R8>(sw); sw = red16(sw, sw); sw = red32(sw, sw);
    const int pb = WS_PART + ((pi * 2 + rh) * 16 + nn) * 2304;
    GAS u64* f2 = (GAS u64*)(p.ws + WS_F2) + ((pi >> 3) * 16 + ((l >> 1) & 3) * 4 + nn) * 16 + ((pi >> 2) & 1) * 2 + (l & 1);
    v4u pa[4], pc[4]; unsigned pe[4];
    unsigned done = 0;
    for (unsigned sp = 0; sp < SPIN_CAP; ++sp) {
      const u64 v = __hip_atomic_load(f2, __ATOMIC_RELAXED, __HIP_MEMORY_SCOPE_AGENT);
      unsigned rdy = (unsigned)__builtin_amdgcn_ballot_w64(v == E);
      rdy &= rdy >> 1;
      if (sp == SPIN_CAP - 1) rdy = 0x55u;
      asm volatile("" ::: "memory");
#pragma unroll
      for (int qq = 0; qq < 4; ++qq)
        if ((rdy & ~done) >> (2 * qq) & 1) {
          pa[qq] = __builtin_amdgcn_raw_buffer_load_b128(rsw, l * 16, pb + qq * 4 * 2304, AUX_SC1);
          pc[qq] = __builtin_amdgcn_raw_buffer_load_b128(rsw, l * 16 + 1024, pb + qq * 4 * 2304, AUX_SC1);
          pe[qq] = __builtin_amdgcn_raw_buffer_load_b32(rsw, 2048 + quad * 64 + row * 8 + half * 4, pb + qq * 4 * 2304, AUX_SC1);
        }
      done |= rdy & 0x55u;
      if (done == 0x55u) break;
      __builtin_amdgcn_s_sleep(PSLEEP);
    }
    STAMP(8);
#pragma unroll
    for (int qq = 0; qq < 4; ++qq) {
#pragma unroll
      for (int i = 0; i < 4; ++i) { h[i] += unp(pa[qq][i]); h[4 + i] += unp(pc[qq][i]); }
      h[8] += unp(pe[qq]);
    }
    STAMP(9);
    float mu, m2, dt;
    {
      const f32x2 t = ((h[0] + h[1]) + (h[2] + h[3])) + ((h[4] + h[5]) + (h[6] + h[7])) + h[8];
      mu = (t[0] + t[1]) * (1.f / 18);
      f32x2 a = {0.f, 0.f}, c = {0.f, 0.f};
#pragma unroll
      for (int i = 0; i < 9; ++i) { const f32x2 d = h[i] - mu; a += d * d; c += h[i] * wv[i]; }
      m2 = a[0] + a[1]; dt = c[0] + c[1];
    }
    {
      const float mo = dppf<DPP_R8>(mu), d = mo - mu;
      m2 = m2 + dppf<DPP_R8>(m2) + d * d * 9.f; mu = 0.5f * (mu + mo); dt += dppf<DPP_R8>(dt);
      auto r = __builtin_amdgcn_permlane16_swap(asu(mu), asu(mu), false, false);
      float e = asf(r[0]) - asf(r[1]);
      m2 = red16(m2, m2) + e * e * 18.f; mu = 0.5f * (asf(r[0]) + asf(r[1])); dt = red16(dt, dt);
      auto r2 = __builtin_amdgcn_permlane32_swap(asu(mu), asu(mu), false, false);
      e = asf(r2[0]) - asf(r2[1]);
      m2 = red32(m2, m2) + e * e * 36.f; mu = 0.5f * (asf(r2[0]) + asf(r2[1])); dt = red32(dt, dt);
    }
    if (l < 8) *(LAS f32x4*)(sst + ((rh * 8 + row) * 4 + nn) * 4) = f32x4{mu, m2, dt, sw};
    WAITL0(); BAR();
    f32x4 st[4];
#pragma unroll
    for (int i = 0; i < 4; ++i) st[i] = *(LAS f32x4*)(sst + (((l & 15) ^ (8 * rh)) * 4 + i) * 4);
    const f32x4 tot = (st[0] + st[1]) + (st[2] + st[3]);
    float mean = tot[0] * 0.25f;
    float dv = 0;
#pragma unroll
    for (int i = 0; i < 4; ++i) { const float d = st[i][0] - mean; dv += d * d; }
    float rstd = __builtin_amdgcn_rsqf((tot[1] + 144.f * dv) * (1.f / D) + 1e-5f);
    const unsigned key = fkey(rstd * (tot[2] - mean * tot[3]));
    unsigned kmax = umax(key, dppu<DPP_X1>(key)); kmax = umax(kmax, dppu<DPP_X2>(kmax)); kmax = umax(kmax, dppu<DPP_HM>(kmax)); kmax = umax(kmax, dppu<DPP_R8>(kmax));
    unsigned hit = (unsigned)__builtin_amdgcn_ballot_w64(key == kmax) & 0xffffu;
    if (rh) hit = ((hit >> 8) | (hit << 8)) & 0xffffu;
    const int brow = __builtin_ctz(hit);
    if (tid < 8) __builtin_amdgcn_raw_buffer_store_b128(v4u{kmax, tag, (unsigned)(pi * 16 + brow), tag}, rsw, tid * 2048, WS_GRAN + pi * 16, AUX_SC1);
    { const float mo = dppf<DPP_R8>(mean), rs2 = dppf<DPP_R8>(rstd); if (l & 8) { mean = mo; rstd = rs2; } }
    f32x2 hn[9];
#pragma unroll
    for (int i = 0; i < 9; ++i) hn[i] = (h[i] - mean) * rstd;
    if (rh * 8 + row == brow) {
      const int co = nn * 576 + (4 * half + quad) * 32, cbase = WS_CAND + pi * 2304;
      __builtin_amdgcn_raw_buffer_store_b128(v4u{pk(hn[0]), tag, pk(hn[1]), tag}, rsw, co, cbase, AUX_SC1);
      __builtin_amdgcn_raw_buffer_store_b128(v4u{pk(hn[2]), tag, pk(hn[3]), tag}, rsw, co + 16, cbase, AUX_SC1);
      __builtin_amdgcn_raw_buffer_store_b128(v4u{pk(hn[4]), tag, pk(hn[5]), tag}, rsw, co + 256, cbase, AUX_SC1);
      __builtin_amdgcn_raw_buffer_store_b128(v4u{pk(hn[6]), tag, pk(hn[7]), tag}, rsw, co + 272, cbase, AUX_SC1);
      __builtin_amdgcn_raw_buffer_store_b64(v2u{pk(hn[8]), tag}, rsw, nn * 576 + 512 + (2 * quad + half) * 8, cbase, AUX_SC1);
    }
    STAMP(10);
#pragma unroll
    for (int i = 0; i < 9; ++i) h[i] += hn[i];
    __builtin_amdgcn_raw_buffer_store_b128(v4u{pk(h[0]), pk(h[1]), pk(h[2]), pk(h[3])}, rso, ro + ea, pi * BSLOT, AUX_SC1);
    __builtin_amdgcn_raw_buffer_store_b128(v4u{pk(h[4]), pk(h[5]), pk(h[6]), pk(h[7])}, rso, ro + ea + 128, pi * BSLOT, AUX_SC1);
    __builtin_amdgcn_raw_buffer_store_b32(pk(h[8]), rso, ro + ee, pi * BSLOT, AUX_SC1);
    WAITV(0); BAR();
    STAMP(11);
    if (tid == 0) __hip_atomic_store((GAS u64*)(p.ws + WS_F3) + pi, E, __ATOMIC_RELAXED, __HIP_MEMORY_SCOPE_AGENT);
    STAMP_FLUSH();
    return;
  }
  if (!is_f && rb >= 36) { STAMP_FLUSH(); return; }
  LAS unsigned* swin = (LAS unsigned*)(L + L_SC);
  LAS float* sgu = (LAS float*)(L + L_SC + 1024);
  LAS float* sy = (LAS float*)(L);
  if (w == 4) {
    v4u g0, g1;
    for (unsigned sp = 0; sp < SPIN_CAP; ++sp) {
      asm volatile("" ::: "memory");
      g0 = __builtin_amdgcn_raw_buffer_load_b128(rsw, l * 16, WS_GRAN + xcd * 2048, AUX_SC1);
      g1 = __builtin_amdgcn_raw_buffer_load_b128(rsw, l * 16 + 1024, WS_GRAN + xcd * 2048, AUX_SC1);
      if (__all(g0[1] == tag && g0[3] == tag && g1[1] == tag && g1[3] == tag)) break;
      __builtin_amdgcn_s_sleep(PSLEEP);
    }
    unsigned k = g0[0], id = g0[2];
    if (g1[0] > k) { k = g1[0]; id = g1[2]; }
    unsigned km = umax(k, dppu<DPP_X1>(k)); km = umax(km, dppu<DPP_X2>(km)); km = umax(km, dppu<DPP_HM>(km)); km = umax(km, dppu<DPP_R8>(km));
    { auto r = __builtin_amdgcn_permlane16_swap(km, km, false, false); km = umax(r[0], r[1]); }
    { auto r = __builtin_amdgcn_permlane32_swap(km, km, false, false); km = umax(r[0], r[1]); }
    const int src = __builtin_ctzll(__builtin_amdgcn_ballot_w64(k == km));
    if (l == src) { swin[0] = km; swin[1] = id & 2047u; }
    WAITL0();
  }
  BAR();
  STAMP(12);
  const int wrow = __builtin_amdgcn_readfirstlane(swin[1]);
  if (is_f) {
    const int cb = WS_CAND + (wrow >> 4) * 2304 + (w >> 1) * 576;
    const int ci = 8 * (w & 1) + (l >> 3);
    v4u xa, xb; v2u xc;
    for (unsigned sp = 0; sp < SPIN_CAP; ++sp) {
      asm volatile("" ::: "memory");
      xa = __builtin_amdgcn_raw_buffer_load_b128(rsw, ci * 32, cb, AUX_SC1);
      xb = __builtin_amdgcn_raw_buffer_load_b128(rsw, ci * 32 + 16, cb, AUX_SC1);
      xc = __builtin_amdgcn_raw_buffer_load_b64(rsw, 512 + (ci >> 1) * 8, cb, AUX_SC1);
      if (__all(xa[1] == tag && xa[3] == tag && xb[1] == tag && xb[3] == tag && xc[1] == tag)) break;
    }
    const v4u xv = {xa[0], xa[2], xb[0], xb[2]};
    const unsigned x8 = (ci & 1) ? xc[0] >> 16 : xc[0] & 0xffffu;
    STAMP(13);
    float g[8], u[8];
#pragma unroll
    for (int i = 0; i < 8; ++i) g[i] = u[i] = 0.f;
#pragma unroll
    for (int k = 0; k < 9; ++k) {
      unsigned xlo, xhi;
      if (k < 8) { const unsigned t = xv[k >> 1]; xlo = (k & 1) ? t >> 16 : t & 0xffffu; xhi = (k & 1) ? t & 0xffff0000u : t << 16; }
      else { xlo = x8; xhi = x8 << 16; }
#pragma unroll
      for (int i = 0; i < 4; ++i) {
        const unsigned a = wg[k][i], c = wu[k][i];
        g[2 * i] = dot2(a, xlo, g[2 * i]); g[2 * i + 1] = dot2(a, xhi, g[2 * i + 1]);
        u[2 * i] = dot2(c, xlo, u[2 * i]); u[2 * i + 1] = dot2(c, xhi, u[2 * i + 1]);
      }
    }
    float t8[8];
#pragma unroll
    for (int i = 0; i < 8; ++i) t8[i] = red32(g[i], u[i]);
#pragma unroll
    for (int i = 0; i < 4; ++i) {
      float v = red16(t8[i], t8[i + 4]); v += dppf<DPP_R8>(v);
      if ((l & 8) == 0) sgu[((8 * (l & 7) + 4 * ((l >> 4) & 1) + i) * 2 + (l >> 5)) * 8 + w] = v;
    }
    WAITL0(); BAR();
    STAMP(14);
    const int f = 8 * w + (l >> 3);
    const f32x4 ga = *(LAS f32x4*)(sgu + f * 16) + *(LAS f32x4*)(sgu + f * 16 + 4), ua = *(LAS f32x4*)(sgu + f * 16 + 8) + *(LAS f32x4*)(sgu + f * 16 + 12);
    const float gg = (ga[0] + ga[1]) + (ga[2] + ga[3]), uu = (ua[0] + ua[1]) + (ua[2] + ua[3]);
    const float gu = gg * __builtin_amdgcn_rcpf(1.f + __expf(-gg)) * uu;
    const unsigned gb = pack2(gu, gu);
    float y[8], z[8];
#pragma unroll
    for (int i = 0; i < 8; ++i) y[i] = 0.f;
#pragma unroll
    for (int k = 0; k < 8; ++k) {
      const unsigned sv = __builtin_amdgcn_readlane(gb, k * 8);
      const unsigned lo = sv & 0xffffu, hi = sv & 0xffff0000u;
#pragma unroll
      for (int i = 0; i < 4; ++i) { const unsigned a = wd[k][i]; y[2 * i] = dot2(a, lo, y[2 * i]); y[2 * i + 1] = dot2(a, hi, y[2 * i + 1]); }
    }
    const float gf = bfhi(gb);
#pragma unroll
    for (int i = 0; i < 4; ++i) { const unsigned a = wd[8][i]; z[2 * i] = bflo(a) * gf; z[2 * i + 1] = bfhi(a) * gf; }
    float z4[4]; f32x2 z2;
#pragma unroll
    for (int i = 0; i < 4; ++i) z4[i] = red32(z[i], z[i + 4]);
#pragma unroll
    for (int i = 0; i < 2; ++i) { float v = red16(z4[i], z4[i + 2]); v += dppf<DPP_R8>(v); z2[i] = v; }
    *(LAS f32x4*)(sy + w * 576 + l * 8) = f32x4{y[0], y[1], y[2], y[3]};
    *(LAS f32x4*)(sy + w * 576 + l * 8 + 4) = f32x4{y[4], y[5], y[6], y[7]};
    if ((l & 8) == 0) *(LAS f32x2*)(sy + w * 576 + 512 + (l & 7) * 8 + (l >> 5) * 4 + ((l >> 4) & 1) * 2) = z2;
    WAITL0(); BAR();
    STAMP(15);
    if (tid < 288) {
      f32x2 v = *(LAS f32x2*)(sy + 2 * tid);
#pragma unroll
      for (int k = 1; k < 8; ++k) v += *(LAS f32x2*)(sy + k * 576 + 2 * tid);
      __builtin_amdgcn_raw_buffer_store_b128(v4u{asu(v[0]), 1u, asu(v[1]), 1u}, rsw, (tid >> 3) * 9216 + (tid & 7) * 16, WS_YG + fb * 128, AUX_SC1);
    }
  } else {
    float a0 = 0, a1 = 0;
    unsigned base_out = 0;
    if (w == 0) {
      for (unsigned sp = 0; sp < SPIN_CAP; ++sp)
        if (__hip_atomic_load((GAS u64*)(p.ws + WS_F3) + (wrow >> 4), __ATOMIC_RELAXED, __HIP_MEMORY_SCOPE_AGENT) == E) break;
      asm volatile("" ::: "memory");
      if (tid < 8) base_out = __builtin_amdgcn_raw_buffer_load_b32(rso, tid * 4, wrow * 1152 + rb * 32, AUX_SC1);
    }
    if (w >= 1 && w < 4) {
      v4u g[3];
      for (unsigned sp = 0; sp < SPIN_CAP; ++sp) {
        asm volatile("" ::: "memory");
#pragma unroll
        for (int k = 0; k < 3; ++k) g[k] = __builtin_amdgcn_raw_buffer_load_b128(rsw, l * 16 + k * 1024, WS_YG + rb * 9216 + (w - 1) * 3072, AUX_SC1);
        if (__all((g[0][1] & g[0][3] & g[1][1] & g[1][3] & g[2][1] & g[2][3]) == 1u)) break;
      }
#pragma unroll
      for (int k = 0; k < 3; ++k) { const unsigned t0 = g[k][0], t1 = g[k][2]; a0 += asf(t0); a1 += asf(t1); }
      float a = red32(a0, a1); a = red16(a, a); a += dppf<DPP_R8>(a);
      if ((l & 24) == 0) sy[(w - 1) * 16 + 2 * (l & 7) + (l >> 5)] = a;
      WAITL0();
    }
    BAR();
    STAMP(16);
    if (tid < 8) {
      const f32x2 y = *(LAS f32x2*)(sy + 2 * tid) + *(LAS f32x2*)(sy + 16 + 2 * tid) + *(LAS f32x2*)(sy + 32 + 2 * tid);
      const float sg = __builtin_amdgcn_rcpf(1.f + __expf(-unkey(swin[0])));
      __builtin_amdgcn_raw_buffer_store_b32(pack2(bflo(base_out) + y[0] * sg, bfhi(base_out) + y[1] * sg), rso, tid * 4, wrow * 1152 + rb * 32, AUX_SC1);
      if (rb == 0 && tid == 0) __hip_atomic_store((GAS u64*)(p.ws + WS_EPOCH), E, __ATOMIC_RELAXED, __HIP_MEMORY_SCOPE_AGENT);
    }
  }
  STAMP(17);
  STAMP_FLUSH();
}

extern "C" void kernel_launch(void* const* d_in, const int* in_sizes, int n_in, void* d_out, int out_size, void* d_ws,
                              size_t ws_size, hipStream_t stream) {
  Params p;
  p.x = (const char*)d_in[0]; p.Cr = (const char*)d_in[1]; p.Ci = (const char*)d_in[2];
  p.Dr = (const char*)d_in[3]; p.Di = (const char*)d_in[4]; p.wr = (const char*)d_in[6];
  p.wg = (const char*)d_in[7]; p.wu = (const char*)d_in[8]; p.wd = (const char*)d_in[9];
  p.out = (char*)d_out; p.ws = (char*)d_ws;
  (void)hipFuncSetAttribute((const void*)fused, hipFuncAttributeMaxDynamicSharedMemorySize, LDS_BYTES);
  fused<<<256, 512, LDS_BYTES, stream>>>(p);
}
```

```cpp
#define TOUCH_G 2097152
#include <hip/hip_runtime.h>
#include <hip/hip_bf16.h>
#include <stdint.h>

#define LAS __attribute__((address_space(3)))
#define GAS __attribute__((address_space(1)))
#define CAS __attribute__((address_space(4)))
typedef short bf16x8 __attribute__((ext_vector_type(8)));
typedef __bf16 bf16x2 __attribute__((ext_vector_type(2)));
typedef float f32x4 __attribute__((ext_vector_type(4)));
typedef float f32x2 __attribute__((ext_vector_type(2)));
typedef unsigned v4u __attribute__((ext_vector_type(4)));
typedef unsigned v2u __attribute__((ext_vector_type(2)));
typedef unsigned long long u64;
typedef __amdgpu_buffer_rsrc_t rsrc_t;

constexpr int S = 2048, D = 576, F = 4608;
constexpr int AUX_SC1 = 16;
#define NPOST 0
#define PSLEEP 2
#define AUX_XD 0
#define AUX_C 0
#define AUX_W 0
constexpr int WS_EPOCH = 0;
constexpr int WS_F3 = 4096;
constexpr int WS_GRAN = 8192;
constexpr int WS_F2 = 32768;
constexpr int WS_F1 = 65536;
constexpr int WS_CAND = 131072;
constexpr int WS_YG = WS_CAND + 128 * 2304;
constexpr int WS_BF = 2 << 20;
constexpr int WS_PART = WS_BF + 256 * 18432;
constexpr int WS_END = WS_PART + 256 * 16 * 2304;
constexpr int L_BR = 0, BSLOT = 18432;
constexpr int L_AR = 4 * BSLOT, ASLOT = 16384;
constexpr int L_MISC = L_AR + 9 * 8192;
constexpr int L_DBG = L_MISC + 2048;
constexpr int L_WR = L_MISC + 4096;
constexpr int L_SC = L_WR + 2048;
constexpr int LDS_BYTES = L_SC + 8192;
constexpr int CODE_LINES = 180;
constexpr unsigned SPIN_CAP = 1u << 20;

struct Params {
  const char *x, *Cr, *Ci, *Dr, *Di, *wr, *wg, *wu, *wd;
  char* out;
  char* ws;
};

__device__ __forceinline__ float asf(unsigned u) { return __builtin_bit_cast(float, u); }
__device__ __forceinline__ unsigned asu(float f) { return __builtin_bit_cast(unsigned, f); }
__device__ __forceinline__ float bflo(unsigned b) { return asf(b << 16); }
__device__ __forceinline__ float bfhi(unsigned b) { return asf(b & 0xffff0000u); }
__device__ __forceinline__ unsigned pack2(float lo, float hi) {
  bf16x2 v; v.x = (__bf16)lo; v.y = (__bf16)hi;
  return __builtin_bit_cast(unsigned, v);
}
__device__ __forceinline__ f32x2 unp(unsigned v) { return f32x2{bflo(v), bfhi(v)}; }
__device__ __forceinline__ unsigned pk(f32x2 v) { return pack2(v[0], v[1]); }
__device__ __forceinline__ float dot2(unsigned a, unsigned b, float c) {
  return __builtin_amdgcn_fdot2_f32_bf16(__builtin_bit_cast(bf16x2, a), __builtin_bit_cast(bf16x2, b), c, false);
}
__device__ __forceinline__ f32x4 mfma(v4u a, v4u b, f32x4 c) {
  return __builtin_amdgcn_mfma_f32_16x16x32_bf16(__builtin_bit_cast(bf16x8, a), __builtin_bit_cast(bf16x8, b), c, 0, 0, 0);
}
template <int CTRL> __device__ __forceinline__ unsigned dppu(unsigned v) { return (unsigned)__builtin_amdgcn_update_dpp(0, (int)v, CTRL, 0xf, 0xf, true); }
template <int CTRL> __device__ __forceinline__ float dppf(float v) { return asf(dppu<CTRL>(asu(v))); }
constexpr int DPP_X1 = 0xB1, DPP_X2 = 0x4E, DPP_HM = 0x141, DPP_R4 = 0x124, DPP_R8 = 0x128;
__device__ __forceinline__ float red16(float a, float b) { auto r = __builtin_amdgcn_permlane16_swap(asu(a), asu(b), false, false); return asf(r[0]) + asf(r[1]); }
__device__ __forceinline__ float red32(float a, float b) { auto r = __builtin_amdgcn_permlane32_swap(asu(a), asu(b), false, false); return asf(r[0]) + asf(r[1]); }
__device__ __forceinline__ rsrc_t mkrs(const void* p, unsigned bytes) { return __builtin_amdgcn_make_buffer_rsrc((void*)p, 0, bytes, 0x00020000); }
template <int AUX> __device__ __forceinline__ void dma(rsrc_t rs, LAS char* dst, int voff, int soff) {
  __builtin_amdgcn_raw_ptr_buffer_load_lds(rs, dst, 16, voff, soff, 0, AUX);
}
#define BAR() asm volatile("s_barrier" ::: "memory")
#define WAITV(n) asm volatile("s_waitcnt vmcnt(%0)" ::"n"(n) : "memory")
#define WAITL0() asm volatile("s_waitcnt lgkmcnt(0)" ::: "memory")
#define SCHED() __builtin_amdgcn_sched_barrier(0)
#define LDSV4(p) (*(LAS v4u*)(p))
#define SGB(m, n) __builtin_amdgcn_sched_group_barrier(m, n, 0)

#define STAMP(i)
#define STAMP_INIT()
#define STAMP_FLUSH()

__device__ __forceinline__ unsigned fkey(float r) { const unsigned u = asu(r); return (u & 0x80000000u) ? ~u : (u | 0x80000000u); }
__device__ __forceinline__ float unkey(unsigned k) { return asf((k & 0x80000000u) ? (k & 0x7fffffffu) : ~k); }
__device__ __forceinline__ unsigned umax(unsigned a, unsigned b) { return a > b ? a : b; }

__global__ __launch_bounds__(512, 2) void fused(Params p) {
  extern __shared__ __attribute__((aligned(16))) char smem[];
  LAS char* L = (LAS char*)smem;
  u64 pc0;
  asm volatile("s_getpc_b64 %0" : "=s"(pc0));
  const int b = blockIdx.x, tid = threadIdx.x, l = tid & 63, w = __builtin_amdgcn_readfirstlane(tid >> 6);
  const int xcd = b & 7, jj = b >> 3, q = xcd >> 1, mh = xcd & 1, mp = jj >> 2, n = jj & 3, m = mh * 8 + mp;
  const int sig = (2 * mp + mh + 4 * n) & 15, panel = q * 4 + n;
  const rsrc_t rsw = mkrs(p.ws, WS_END);
  u64 E;
#define LOAD_EPOCH() do { asm volatile("s_load_dwordx2 %0, %1, 0x0\n s_waitcnt lgkmcnt(0)" : "=s"(E) : "s"(p.ws) : "memory"); E += 1ull; } while (0)
  const bool is_f = jj < 9, is_p = jj >= 9 && jj < 25;
  const int fb = xcd * 9 + jj, pi = xcd * 16 + jj - 9, rb = (jj - 25) * 8 + xcd;
  asm volatile("" :: "s"(p.x), "s"(p.Dr), "s"(p.ws));
  v4u wg[9], wu[9], wd[9];
  STAMP_INIT(); STAMP(0);

  unsigned tch = 0;
  if (w == 2) {
    const int to = (l * 32 + jj) * TOUCH_G;
    tch = __builtin_amdgcn_raw_buffer_load_b32(mkrs(p.Cr, S * S * 2), to, 0, 0) ^ __builtin_amdgcn_raw_buffer_load_b32(mkrs(p.Ci, S * S * 2), to, 0, 0)
        ^ __builtin_amdgcn_raw_buffer_load_b32(mkrs(p.wg, D * F * 2), to, 0, 0) ^ __builtin_amdgcn_raw_buffer_load_b32(mkrs(p.wu, D * F * 2), to, 0, 0)
        ^ __builtin_amdgcn_raw_buffer_load_b32(mkrs(p.wd, D * F * 2), to, 0, 0) ^ __builtin_amdgcn_raw_buffer_load_b32(mkrs(p.out, S * D * 2), to, 0, 0)
        ^ __builtin_amdgcn_raw_buffer_load_b32(mkrs(p.x, S * D * 2), to, 0, 0) ^ __builtin_amdgcn_raw_buffer_load_b32(mkrs(p.wr, D * 2), to, 0, 0)
        ^ __builtin_amdgcn_raw_buffer_load_b32(mkrs(p.Dr, D * D * 2), to, 0, 0) ^ __builtin_amdgcn_raw_buffer_load_b32(mkrs(p.Di, D * D * 2), to, 0, 0)
        ^ __builtin_amdgcn_raw_buffer_load_b32(rsw, to, 0, AUX_SC1);
    asm volatile("" :: "v"(tch));
  }
#define LOADW_GU(dst, ptr) do { const rsrc_t r_ = mkrs(ptr, D * F * 2); \
    _Pragma("unroll") for (int k = 0; k < 8; ++k) dst[k] = __builtin_amdgcn_raw_buffer_load_b128(r_, (l >> 3) * 8 * (F * 2) + (l & 7) * 16, (144 * (w >> 1) + 64 * (w & 1) + k) * (F * 2) + fb * 128, AUX_W); \
    dst[8] = __builtin_amdgcn_raw_buffer_load_b128(r_, (l >> 3) * (F * 2) + (l & 7) * 16, (144 * (w >> 1) + 128 + 8 * (w & 1)) * (F * 2) + fb * 128, AUX_W); } while (0)
#define LOADW_G() LOADW_GU(wg, p.wg)
#define LOADW_U() LOADW_GU(wu, p.wu)
#define LOADW_D() do { const rsrc_t r_ = mkrs(p.wd, D * F * 2); \
    _Pragma("unroll") for (int k = 0; k < 8; ++k) wd[k] = __builtin_amdgcn_raw_buffer_load_b128(r_, l * 16, (fb * 64 + w * 8 + k) * 1152, AUX_W); \
    wd[8] = __builtin_amdgcn_raw_buffer_load_b128(r_, (l >> 3) * 1152 + 1024 + (l & 7) * 16, (fb * 64 + w * 8) * 1152, AUX_W); } while (0)

  if (__builtin_expect(w >= 4, 1)) {
    const int lw = w & 1;
    const int rperm = 8 * (l >> 5) + ((l >> 3) & 3), rlin = l >> 3;
    const int c0 = ((l & 7) ^ (l >> 4)) * 16, c1 = c0 ^ 64;
    int ko = mp * 128;
    if (w < 6) {
      {
        const rsrc_t rsd = mkrs((q >> 1) ? p.Di : p.Dr, D * D * 2);
        int vk[9], sk[9];
#pragma unroll
        for (int k = 0; k < 9; ++k) {
          const int P = lw * 9 + k, j = P >> 1, odd = P & 1;
          const int sc = j < 8 ? 32 * (j >> 1) + 4 * (j & 1) + 16 * odd : 128 + 8 * odd;
          sk[k] = (n * 144 + sc) * 1152;
          vk[k] = (j < 8 ? rperm : rlin) * 1152 + (odd ? c1 : c0);
        }
#define ISSUED(st) do { \
          _Pragma("unroll") for (int k = 0; k < 9; ++k) dma<AUX_XD>(rsd, L + L_BR + ((st) & 3) * BSLOT + lw * 9216 + k * 1024, vk[k], sk[k] + ko); \
          ko = ko == 1024 ? 0 : ko + 128; } while (0)
        STAMP(18);
        ISSUED(0); ISSUED(1);
        LOAD_EPOCH();
        WAITV(9); STAMP(21); BAR();
        ISSUED(2); ISSUED(3); WAITV(18); BAR();
#pragma unroll 1
        for (int j = 2; j < 7; ++j) { ISSUED(j + 2); WAITV(18); BAR(); }
        WAITV(9); BAR();
        WAITV(0); STAMP(31); BAR();
      }
      BAR();
      BAR();
      STAMP(1);
      const int pbase = WS_BF + panel * 16 * BSLOT + lw * 9216;
      GAS u64* f1 = (GAS u64*)(p.ws + WS_F1) + (panel * 2 + lw) * 256 + ((l & 31) >> 1) * 16 + (l & 1);
      unsigned have = 0;
      __builtin_amdgcn_s_sleep(20);
#define NEED(st) do { const unsigned bit_ = 3u << (2 * ((sig + (st)) & 15)); \
        for (unsigned sp_ = 0; (have & bit_) != bit_ && sp_ < SPIN_CAP; ++sp_) { \
          const u64 v_ = __hip_atomic_load(f1, __ATOMIC_RELAXED, __HIP_MEMORY_SCOPE_AGENT); \
          have = (unsigned)__builtin_amdgcn_ballot_w64(v_ == E); if ((have & bit_) != bit_) __builtin_amdgcn_s_sleep(PSLEEP); } } while (0)
#define ISSUEB(st) do { NEED(st); const int so_ = pbase + ((sig + (st)) & 15) * BSLOT; \
        _Pragma("unroll") for (int k = 0; k < 9; ++k) dma<AUX_SC1>(rsw, L + L_BR + ((st) & 3) * BSLOT + lw * 9216 + k * 1024, l * 16, so_ + k * 1024); } while (0)
      ISSUEB(1);
      STAMP(3);
      ISSUEB(2);
      WAITV(9); BAR();
      STAMP(4);
      ISSUEB(3);
#pragma unroll 1
      for (int j = 2; j < 14; ++j) { ISSUEB(j + 2); WAITV(18); BAR(); }
    } else {
      const rsrc_t rsx = mkrs(p.x, S * D * 2);
      {
        const int vx0 = rperm * 1152 + c0, vx1 = rperm * 1152 + c1, sx = (((q & 1) * 16 + sig) * 64 + 32 * lw) * 1152;
#define ISSUEX(st) do { \
          _Pragma("unroll") for (int k = 0; k < 4; ++k) dma<AUX_XD>(rsx, L + L_AR + (st) * 8192 + lw * 4096 + k * 1024, (k & 1) ? vx1 : vx0, sx + (4 * (k >> 1) + 16 * (k & 1)) * 1152 + ko); \
          ko = ko == 1024 ? 0 : ko + 128; } while (0)
        ISSUEX(0); ISSUEX(1); ISSUEX(2); ISSUEX(3);
        LOAD_EPOCH();
        WAITV(12); BAR();
        WAITV(8); BAR();
#pragma unroll 1
        for (int j = 2; j < 7; ++j) { ISSUEX(j + 2); WAITV(8); BAR(); }
      }
      const rsrc_t rsc = mkrs((q >> 1) ? p.Ci : p.Cr, S * S * 2);
      const int v0 = (l >> 3) * 4096 + c0, v1 = v0 ^ 64;
      const int sb = (m * 128 + lw * 64) * 4096 + (q & 1) * 2048;
#define ISSUEA(st) do { const int so_ = sb + ((sig + (st)) & 15) * 128; \
        _Pragma("unroll") for (int k = 0; k < 8; ++k) dma<AUX_C>(rsc, L + L_AR + ((st) & 3) * ASLOT + lw * 8192 + k * 1024, (k & 1) ? v1 : v0, so_ + k * 8 * 4096); } while (0)
      ISSUEA(0);
      WAITV(12); BAR();
      ISSUEA(1);
      WAITV(16); BAR();
      BAR();
      WAITV(8); BAR();
      WAITV(8 * NPOST); BAR();
      ISSUEA(2);
      ISSUEA(3);
#pragma unroll 1
      for (int j = 2; j < 14; ++j) { ISSUEA(j + 2); WAITV(16); BAR(); }
    }
    if (is_p) {
      if (w < 6) {
        const rsrc_t rsx = mkrs(p.x, S * D * 2);
#pragma unroll
        for (int k = 0; k < 9; ++k) dma<0>(rsx, L + L_BR + lw * 9216 + k * 1024, l * 16, pi * BSLOT + lw * 9216 + k * 1024);
        WAITV(18); BAR(); WAITV(9); BAR();
      } else {
        dma<0>(mkrs(p.wr, D * 2), L + L_WR + lw * 1024, l * 16, lw * 1024);
        WAITV(9); BAR(); WAITV(1); BAR();
      }
      WAITV(0);
      BAR();
    } else if (is_f) {
      LOADW_G(); SCHED();
      if (w < 6) WAITV(18); else WAITV(17);
      BAR(); WAITV(9); BAR();
      BAR();
      LOADW_U(); LOADW_D(); SCHED();
    } else {
      if (w < 6) WAITV(9); else WAITV(8);
      BAR(); WAITV(0); BAR();
    }
    STAMP(5);
  } else {
    if (tid < 81) __builtin_amdgcn_raw_buffer_store_b128(v4u{0, 0, 0, 0}, rsw, tid * 16, WS_YG + b * 1296, AUX_SC1);
    unsigned warm = 0;
    if (w == 3 && l < 8 && jj * 8 + l < CODE_LINES) warm = *(const GAS unsigned*)((pc0 & ~255ull) + (jj * 8 + l) * 128);
    LOAD_EPOCH();
    const int wa = w >> 1, wn = w & 1;
    const int o0 = (l & 15) * 128 + (((l >> 4) ^ ((l & 15) >> 1)) * 16), o1 = o0 ^ 64;
    {
      f32x4 acc[2][4], acc8 = {0, 0, 0, 0}, cs0 = {0, 0, 0, 0}, cs1 = {0, 0, 0, 0}, cs8 = {0, 0, 0, 0};
      const v4u ones = {0x3f803f80u, 0x3f803f80u, 0x3f803f80u, 0x3f803f80u};
#pragma unroll
      for (int t = 0; t < 2; ++t)
#pragma unroll
        for (int j = 0; j < 4; ++j) acc[t][j] = f32x4{0, 0, 0, 0};
      f32x4 sx = {0, 0, 0, 0}, gx = {0, 0, 0, 0};
      v4u x0[2], d0[5], x1[2], d1[5];
#define RD1(xf, df, st, o) do { \
        _Pragma("unroll") for (int t = 0; t < 2; ++t) xf[t] = LDSV4(L + L_AR + (st) * 8192 + (2 * wa + (t ^ wn)) * 2048 + (o)); \
        _Pragma("unroll") for (int j = 0; j < 4; ++j) df[j] = LDSV4(L + L_BR + ((st) & 3) * BSLOT + (4 * wn + (j ^ (2 * wa))) * 2048 + (o)); \
        df[4] = LDSV4(L + L_BR + ((st) & 3) * BSLOT + 8 * 2048 + (o)); } while (0)
#define MM1(xf, df) do { \
        cs0 = mfma(ones, df[0], cs0); cs1 = mfma(ones, df[1], cs1); cs8 = mfma(ones, df[4], cs8); sx = mfma(ones, xf[0], sx); gx = mfma(xf[0], xf[0], gx); \
        _Pragma("unroll") for (int t = 0; t < 2; ++t) _Pragma("unroll") for (int j = 0; j < 4; ++j) acc[t][j] = mfma(xf[t], df[j], acc[t][j]); \
        acc8 = mfma(xf[0], df[4], acc8); } while (0)
#define ILV1() do { _Pragma("unroll") for (int z = 0; z < 7; ++z) { SGB(0x008, 1); SGB(0x100, 1); } SGB(0x008, 7); SCHED(); } while (0)
      BAR();
      STAMP(1);
      RD1(x0, d0, 0, o0); RD1(x1, d1, 0, o1); MM1(x0, d0); SCHED(); WAITL0();
#pragma unroll
      for (int st = 1; st < 9; ++st) {
        BAR();
        RD1(x0, d0, st, o0); MM1(x1, d1); ILV1();
        RD1(x1, d1, st, o1); MM1(x0, d0); ILV1();
        WAITL0();
      }
      MM1(x1, d1);
      STAMP(2);
      LAS float* smean = (LAS float*)(L + L_MISC);
      LAS float* srstd = smean + 64;
      LAS float* scs = smean + 128;
      if (((l & 15) >> 2) == (l >> 4)) {
        const float s2 = (l & 2) ? ((l & 1) ? gx[3] : gx[2]) : ((l & 1) ? gx[1] : gx[0]);
        const float mean = sx[0] * (1.f / D);
        const float var = fmaxf(s2 * (1.f / D) - mean * mean, 0.f);
        const float rs = __builtin_amdgcn_rsqf(var + 1e-5f);
        smean[w * 16 + (l & 15)] = mean;
        srstd[w * 16 + (l & 15)] = (q >> 1) ? -rs : rs;
        scs[(4 * wn + 2 * wa) * 16 + (l & 15)] = cs0[0];
        scs[(4 * wn + 2 * wa + 1) * 16 + (l & 15)] = cs1[0];
      }
      WAITL0(); BAR();
      f32x4 mean[2], rstd[2];
#pragma unroll
      for (int t = 0; t < 2; ++t) {
        mean[t] = *(LAS f32x4*)(smean + (2 * wa + (t ^ wn)) * 16 + 4 * (l >> 4));
        rstd[t] = *(LAS f32x4*)(srstd + (2 * wa + (t ^ wn)) * 16 + 4 * (l >> 4));
      }
      LAS char* ob = L + L_BR + wa * 9216 + l * 16;
      const int gb = WS_BF + (panel * 16 + sig) * BSLOT + wa * 9216;
#pragma unroll
      for (int j = 0; j < 4; ++j) {
        const int jg = 4 * wn + (j ^ (2 * wa));
        const float c = j == 0 ? cs0[0] : j == 1 ? cs1[0] : scs[jg * 16 + (l & 15)];
        const f32x4 v0 = (acc[0][j] - mean[0] * c) * rstd[0], v1 = (acc[1][j] - mean[1] * c) * rstd[1];
        const unsigned p0 = pack2(v0[0], v0[1]), p1 = pack2(v0[2], v0[3]), p2 = pack2(v1[0], v1[1]), p3 = pack2(v1[2], v1[3]);
        const v4u o = {wn ? p2 : p0, wn ? p3 : p1, wn ? p0 : p2, wn ? p1 : p3};
        __builtin_amdgcn_raw_buffer_store_b128(o, rsw, l * 16, gb + jg * 1024, AUX_SC1);
        LDSV4(ob + jg * 1024) = o;
      }
      {
        const f32x4 v = (acc8 - mean[0] * cs8[0]) * rstd[0];
        const v2u o = {pack2(v[0], v[1]), pack2(v[2], v[3])};
        __builtin_amdgcn_raw_buffer_store_b64(o, rsw, l * 16, gb + 8 * 1024 + wn * 8, AUX_SC1);
        *(LAS v2u*)(ob + 8 * 1024 + wn * 8) = o;
      }
      WAITL0(); BAR();
    }
    STAMP(3);
    const int oa0 = o0 + wa * 8192, oa1 = o1 + wa * 8192, ob = l * 16 + wn * 4096, ob8 = l * 16 + 8192;
    f32x4 acc[4][4], acc8[2];
#pragma unroll
    for (int i = 0; i < 4; ++i)
#pragma unroll
      for (int j = 0; j < 4; ++j) acc[i][j] = f32x4{0, 0, 0, 0};
    acc8[0] = acc8[1] = f32x4{0, 0, 0, 0};
    v4u c0[4], a0[5], c1[4], a1[5];
#define RD2(c, a, slot, ks, o) do { \
      _Pragma("unroll") for (int i = 0; i < 4; ++i) { a[i] = LDSV4(L + L_BR + (slot) * BSLOT + (ks) * 9216 + ob + i * 1024); c[i] = LDSV4(L + L_AR + (slot) * ASLOT + (o) + (i ^ (2 * wn)) * 2048); } \
      a[4] = LDSV4(L + L_BR + (slot) * BSLOT + (ks) * 9216 + ob8); } while (0)
#define MM2(c, a) do { \
      _Pragma("unroll") for (int i = 0; i < 4; ++i) _Pragma("unroll") for (int j = 0; j < 4; ++j) acc[i][j] = mfma(a[j], c[i], acc[i][j]); \
      acc8[0] = mfma(a[4], c[0], acc8[0]); acc8[1] = mfma(a[4], c[1], acc8[1]); } while (0)
#define ILV2() do { _Pragma("unroll") for (int z = 0; z < 9; ++z) { SGB(0x008, 1); SGB(0x100, 1); } SGB(0x008, 9); SCHED(); } while (0)
#define STEP2(slot) do { BAR(); RD2(c0, a0, slot, 0, oa0); MM2(c1, a1); ILV2(); RD2(c1, a1, slot, 1, oa1); MM2(c0, a0); ILV2(); WAITL0(); } while (0)
    RD2(c0, a0, 0, 0, oa0); RD2(c1, a1, 0, 1, oa1); MM2(c0, a0); SCHED(); WAITL0();
    WAITV(0);
    asm volatile("" :: "v"(warm));
    if (l == 0) __hip_atomic_store((GAS u64*)(p.ws + WS_F1) + ((panel * 2 + wa) * 16 + sig) * 16 + wn, E, __ATOMIC_RELAXED, __HIP_MEMORY_SCOPE_AGENT);
    STAMP(19);
    STEP2(1);
    STAMP(4);
    STEP2(2); STEP2(3);
#pragma unroll 1
    for (int it = 0; it < 3; ++it) { STEP2(0); STEP2(1); STEP2(2); STEP2(3); }
    MM2(c1, a1);
    STAMP(5);
    const int t = l & 15, quad = l >> 4;
    const int pv = (t >> 3) * (16 * 2304) + quad * 128 + (t & 7) * 16, pv8 = (t >> 3) * (16 * 2304) + 2048 + quad * 64 + (t & 7) * 8;
#pragma unroll
    for (int i = 0; i < 4; ++i) {
      const int so = WS_PART + ((m * 16 + (wa * 4 + (i ^ (2 * wn))) * 2) * 16 + panel) * 2304;
#pragma unroll
      for (int u = 0; u < 2; ++u) {
        const f32x4 y0 = acc[i][2 * u], y1 = acc[i][2 * u + 1];
        __builtin_amdgcn_raw_buffer_store_b128(v4u{pack2(y0[0], y0[1]), pack2(y0[2], y0[3]), pack2(y1[0], y1[1]), pack2(y1[2], y1[3])}, rsw, pv, so + (wn * 2 + u) * 512, AUX_SC1);
      }
      if (i < 2) __builtin_amdgcn_raw_buffer_store_b64(v2u{pack2(acc8[i][0], acc8[i][1]), pack2(acc8[i][2], acc8[i][3])}, rsw, pv8, so, AUX_SC1);
    }
    STAMP(6);
    if (is_p) BAR();
    WAITV(0);
    STAMP(7);
    if (l == 0) __hip_atomic_store((GAS u64*)(p.ws + WS_F2) + (m * 16 + panel) * 16 + w, E, __ATOMIC_RELAXED, __HIP_MEMORY_SCOPE_AGENT);
    SCHED();
    if (is_f) { BAR(); LOADW_G(); LOADW_U(); LOADW_D(); SCHED(); }
    STAMP(20);
  }
  const unsigned tag = (unsigned)E;
  const rsrc_t rso = mkrs(p.out, S * D * 2);
  if (is_p) {
    LAS float* sst = (LAS float*)(L + L_SC);
    const int row = l & 7, quad = (l >> 3) & 3, half = l >> 5, nn = w & 3, rh = w >> 2;
    const int ea = (nn * 144 + 32 * half + 8 * quad) * 2, ee = (nn * 144 + 128 + 4 * quad + 2 * half) * 2, ro = (rh * 8 + row) * 1152;
    f32x2 h[9], wv[9];
    {
      const v4u xa = LDSV4(L + L_BR + ro + ea), xc = LDSV4(L + L_BR + ro + ea + 128), wa4 = LDSV4(L + L_WR + ea), wc4 = LDSV4(L + L_WR + ea + 128);
      const unsigned xe = *(LAS unsigned*)(L + L_BR + ro + ee), we = *(LAS unsigned*)(L + L_WR + ee);
#pragma unroll
      for (int i = 0; i < 4; ++i) { h[i] = unp(xa[i]); h[4 + i] = unp(xc[i]); wv[i] = unp(wa4[i]); wv[4 + i] = unp(wc4[i]); }
      h[8] = unp(xe); wv[8] = unp(we);
    }
    float sw;
    { const f32x2 t = ((wv[0] + wv[1]) + (wv[2] + wv[3])) + ((wv[4] + wv[5]) + (wv[6] + wv[7])) + wv[8]; sw = t[0] + t[1]; }
    sw += dppf<DPP_R8>(sw); sw = red16(sw, sw); sw = red32(sw, sw);
    const int pb = WS_PART + ((pi * 2 + rh) * 16 + nn) * 2304;
    GAS u64* f2 = (GAS u64*)(p.ws + WS_F2) + ((pi >> 3) * 16 + ((l >> 1) & 3) * 4 + nn) * 16 + ((pi >> 2) & 1) * 2 + (l & 1);
    v4u pa[4], pc[4]; unsigned pe[4];
    unsigned done = 0;
    for (unsigned sp = 0; sp < SPIN_CAP; ++sp) {
      const u64 v = __hip_atomic_load(f2, __ATOMIC_RELAXED, __HIP_MEMORY_SCOPE_AGENT);
      unsigned rdy = (unsigned)__builtin_amdgcn_ballot_w64(v == E);
      rdy &= rdy >> 1;
      if (sp == SPIN_CAP - 1) rdy = 0x55u;
      asm volatile("" ::: "memory");
#pragma unroll
      for (int qq = 0; qq < 4; ++qq)
        if ((rdy & ~done) >> (2 * qq) & 1) {
          pa[qq] = __builtin_amdgcn_raw_buffer_load_b128(rsw, l * 16, pb + qq * 4 * 2304, AUX_SC1);
          pc[qq] = __builtin_amdgcn_raw_buffer_load_b128(rsw, l * 16 + 1024, pb + qq * 4 * 2304, AUX_SC1);
          pe[qq] = __builtin_amdgcn_raw_buffer_load_b32(rsw, 2048 + quad * 64 + row * 8 + half * 4, pb + qq * 4 * 2304, AUX_SC1);
        }
      done |= rdy & 0x55u;
      if (done == 0x55u) break;
      __builtin_amdgcn_s_sleep(PSLEEP);
    }
    STAMP(8);
#pragma unroll
    for (int qq = 0; qq < 4; ++qq) {
#pragma unroll
      for (int i = 0; i < 4; ++i) { h[i] += unp(pa[qq][i]); h[4 + i] += unp(pc[qq][i]); }
      h[8] += unp(pe[qq]);
    }
    STAMP(9);
    float mu, m2, dt;
    {
      const f32x2 t = ((h[0] + h[1]) + (h[2] + h[3])) + ((h[4] + h[5]) + (h[6] + h[7])) + h[8];
      mu = (t[0] + t[1]) * (1.f / 18);
      f32x2 a = {0.f, 0.f}, c = {0.f, 0.f};
#pragma unroll
      for (int i = 0; i < 9; ++i) { const f32x2 d = h[i] - mu; a += d * d; c += h[i] * wv[i]; }
      m2 = a[0] + a[1]; dt = c[0] + c[1];
    }
    {
      const float mo = dppf<DPP_R8>(mu), d = mo - mu;
      m2 = m2 + dppf<DPP_R8>(m2) + d * d * 9.f; mu = 0.5f * (mu + mo); dt += dppf<DPP_R8>(dt);
      auto r = __builtin_amdgcn_permlane16_swap(asu(mu), asu(mu), false, false);
      float e = asf(r[0]) - asf(r[1]);
      m2 = red16(m2, m2) + e * e * 18.f; mu = 0.5f * (asf(r[0]) + asf(r[1])); dt = red16(dt, dt);
      auto r2 = __builtin_amdgcn_permlane32_swap(asu(mu), asu(mu), false, false);
      e = asf(r2[0]) - asf(r2[1]);
      m2 = red32(m2, m2) + e * e * 36.f; mu = 0.5f * (asf(r2[0]) + asf(r2[1])); dt = red32(dt, dt);
    }
    if (l < 8) *(LAS f32x4*)(sst + ((rh * 8 + row) * 4 + nn) * 4) = f32x4{mu, m2, dt, sw};
    WAITL0(); BAR();
    f32x4 st[4];
#pragma unroll
    for (int i = 0; i < 4; ++i) st[i] = *(LAS f32x4*)(sst + (((l & 15) ^ (8 * rh)) * 4 + i) * 4);
    const f32x4 tot = (st[0] + st[1]) + (st[2] + st[3]);
    float mean = tot[0] * 0.25f;
    float dv = 0;
#pragma unroll
    for (int i = 0; i < 4; ++i) { const float d = st[i][0] - mean; dv += d * d; }
    float rstd = __builtin_amdgcn_rsqf((tot[1] + 144.f * dv) * (1.f / D) + 1e-5f);
    const unsigned key = fkey(rstd * (tot[2] - mean * tot[3]));
    unsigned kmax = umax(key, dppu<DPP_X1>(key)); kmax = umax(kmax, dppu<DPP_X2>(kmax)); kmax = umax(kmax, dppu<DPP_HM>(kmax)); kmax = umax(kmax, dppu<DPP_R8>(kmax));
    unsigned hit = (unsigned)__builtin_amdgcn_ballot_w64(key == kmax) & 0xffffu;
    if (rh) hit = ((hit >> 8) | (hit << 8)) & 0xffffu;
    const int brow = __builtin_ctz(hit);
    if (tid < 8) __builtin_amdgcn_raw_buffer_store_b128(v4u{kmax, tag, (unsigned)(pi * 16 + brow), tag}, rsw, tid * 2048, WS_GRAN + pi * 16, AUX_SC1);
    { const float mo = dppf<DPP_R8>(mean), rs2 = dppf<DPP_R8>(rstd); if (l & 8) { mean = mo; rstd = rs2; } }
    f32x2 hn[9];
#pragma unroll
    for (int i = 0; i < 9; ++i) hn[i] = (h[i] - mean) * rstd;
    if (rh * 8 + row == brow) {
      const int co = nn * 576 + (4 * half + quad) * 32, cbase = WS_CAND + pi * 2304;
      __builtin_amdgcn_raw_buffer_store_b128(v4u{pk(hn[0]), tag, pk(hn[1]), tag}, rsw, co, cbase, AUX_SC1);
      __builtin_amdgcn_raw_buffer_store_b128(v4u{pk(hn[2]), tag, pk(hn[3]), tag}, rsw, co + 16, cbase, AUX_SC1);
      __builtin_amdgcn_raw_buffer_store_b128(v4u{pk(hn[4]), tag, pk(hn[5]), tag}, rsw, co + 256, cbase, AUX_SC1);
      __builtin_amdgcn_raw_buffer_store_b128(v4u{pk(hn[6]), tag, pk(hn[7]), tag}, rsw, co + 272, cbase, AUX_SC1);
      __builtin_amdgcn_raw_buffer_store_b64(v2u{pk(hn[8]), tag}, rsw, nn * 576 + 512 + (2 * quad + half) * 8, cbase, AUX_SC1);
    }
    STAMP(10);
#pragma unroll
    for (int i = 0; i < 9; ++i) h[i] += hn[i];
    __builtin_amdgcn_raw_buffer_store_b128(v4u{pk(h[0]), pk(h[1]), pk(h[2]), pk(h[3])}, rso, ro + ea, pi * BSLOT, AUX_SC1);
    __builtin_amdgcn_raw_buffer_store_b128(v4u{pk(h[4]), pk(h[5]), pk(h[6]), pk(h[7])}, rso, ro + ea + 128, pi * BSLOT, AUX_SC1);
    __builtin_amdgcn_raw_buffer_store_b32(pk(h[8]), rso, ro + ee, pi * BSLOT, AUX_SC1);
    WAITV(0); BAR();
    STAMP(11);
    if (tid == 0) __hip_atomic_store((GAS u64*)(p.ws + WS_F3) + pi, E, __ATOMIC_RELAXED, __HIP_MEMORY_SCOPE_AGENT);
    STAMP_FLUSH();
    return;
  }
  if (!is_f && rb >= 36) { STAMP_FLUSH(); return; }
  LAS unsigned* swin = (LAS unsigned*)(L + L_SC);
  LAS float* sgu = (LAS float*)(L + L_SC + 1024);
  LAS float* sy = (LAS float*)(L);
  if (w == 4) {
    v4u g0, g1;
    for (unsigned sp = 0; sp < SPIN_CAP; ++sp) {
      asm volatile("" ::: "memory");
      g0 = __builtin_amdgcn_raw_buffer_load_b128(rsw, l * 16, WS_GRAN + xcd * 2048, AUX_SC1);
      g1 = __builtin_amdgcn_raw_buffer_load_b128(rsw, l * 16 + 1024, WS_GRAN + xcd * 2048, AUX_SC1);
      if (__all(g0[1] == tag && g0[3] == tag && g1[1] == tag && g1[3] == tag)) break;
      __builtin_amdgcn_s_sleep(PSLEEP);
    }
    unsigned k = g0[0], id = g0[2];
    if (g1[0] > k) { k = g1[0]; id = g1[2]; }
    unsigned km = umax(k, dppu<DPP_X1>(k)); km = umax(km, dppu<DPP_X2>(km)); km = umax(km, dppu<DPP_HM>(km)); km = umax(km, dppu<DPP_R8>(km));
    { auto r = __builtin_amdgcn_permlane16_swap(km, km, false, false); km = umax(r[0], r[1]); }
    { auto r = __builtin_amdgcn_permlane32_swap(km, km, false, false); km = umax(r[0], r[1]); }
    const int src = __builtin_ctzll(__builtin_amdgcn_ballot_w64(k == km));
    if (l == src) { swin[0] = km; swin[1] = id & 2047u; }
    WAITL0();
  }
  BAR();
  STAMP(12);
  const int wrow = __builtin_amdgcn_readfirstlane(swin[1]);
  if (is_f) {
    const int cb = WS_CAND + (wrow >> 4) * 2304 + (w >> 1) * 576;
    const int ci = 8 * (w & 1) + (l >> 3);
    v4u xa, xb; v2u xc;
    for (unsigned sp = 0; sp < SPIN_CAP; ++sp) {
      asm volatile("" ::: "memory");
      xa = __builtin_amdgcn_raw_buffer_load_b128(rsw, ci * 32, cb, AUX_SC1);
      xb = __builtin_amdgcn_raw_buffer_load_b128(rsw, ci * 32 + 16, cb, AUX_SC1);
      xc = __builtin_amdgcn_raw_buffer_load_b64(rsw, 512 + (ci >> 1) * 8, cb, AUX_SC1);
      if (__all(xa[1] == tag && xa[3] == tag && xb[1] == tag && xb[3] == tag && xc[1] == tag)) break;
    }
    const v4u xv = {xa[0], xa[2], xb[0], xb[2]};
    const unsigned x8 = (ci & 1) ? xc[0] >> 16 : xc[0] & 0xffffu;
    STAMP(13);
    float g[8], u[8];
#pragma unroll
    for (int i = 0; i < 8; ++i) g[i] = u[i] = 0.f;
#pragma unroll
    for (int k = 0; k < 9; ++k) {
      unsigned xlo, xhi;
      if (k < 8) { const unsigned t = xv[k >> 1]; xlo = (k & 1) ? t >> 16 : t & 0xffffu; xhi = (k & 1) ? t & 0xffff0000u : t << 16; }
      else { xlo = x8; xhi = x8 << 16; }
#pragma unroll
      for (int i = 0; i < 4; ++i) {
        const unsigned a = wg[k][i], c = wu[k][i];
        g[2 * i] = dot2(a, xlo, g[2 * i]); g[2 * i + 1] = dot2(a, xhi, g[2 * i + 1]);
        u[2 * i] = dot2(c, xlo, u[2 * i]); u[2 * i + 1] = dot2(c, xhi, u[2 * i + 1]);
      }
    }
    float t8[8];
#pragma unroll
    for (int i = 0; i < 8; ++i) t8[i] = red32(g[i], u[i]);
#pragma unroll
    for (int i = 0; i < 4; ++i) {
      float v = red16(t8[i], t8[i + 4]); v += dppf<DPP_R8>(v);
      if ((l & 8) == 0) sgu[((8 * (l & 7) + 4 * ((l >> 4) & 1) + i) * 2 + (l >> 5)) * 8 + w] = v;
    }
    WAITL0(); BAR();
    STAMP(14);
    const int f = 8 * w + (l >> 3);
    const f32x4 ga = *(LAS f32x4*)(sgu + f * 16) + *(LAS f32x4*)(sgu + f * 16 + 4), ua = *(LAS f32x4*)(sgu + f * 16 + 8) + *(LAS f32x4*)(sgu + f * 16 + 12);
    const float gg = (ga[0] + ga[1]) + (ga[2] + ga[3]), uu = (ua[0] + ua[1]) + (ua[2] + ua[3]);
    const float gu = gg * __builtin_amdgcn_rcpf(1.f + __expf(-gg)) * uu;
    const unsigned gb = pack2(gu, gu);
    float y[8], z[8];
#pragma unroll
    for (int i = 0; i < 8; ++i) y[i] = 0.f;
#pragma unroll
    for (int k = 0; k < 8; ++k) {
      const unsigned sv = __builtin_amdgcn_readlane(gb, k * 8);
      const unsigned lo = sv & 0xffffu, hi = sv & 0xffff0000u;
#pragma unroll
      for (int i = 0; i < 4; ++i) { const unsigned a = wd[k][i]; y[2 * i] = dot2(a, lo, y[2 * i]); y[2 * i + 1] = dot2(a, hi, y[2 * i + 1]); }
    }
    const float gf = bfhi(gb);
#pragma unroll
    for (int i = 0; i < 4; ++i) { const unsigned a = wd[8][i]; z[2 * i] = bflo(a) * gf; z[2 * i + 1] = bfhi(a) * gf; }
    float z4[4]; f32x2 z2;
#pragma unroll
    for (int i = 0; i < 4; ++i) z4[i] = red32(z[i], z[i + 4]);
#pragma unroll
    for (int i = 0; i < 2; ++i) { float v = red16(z4[i], z4[i + 2]); v += dppf<DPP_R8>(v); z2[i] = v; }
    *(LAS f32x4*)(sy + w * 576 + l * 8) = f32x4{y[0], y[1], y[2], y[3]};
    *(LAS f32x4*)(sy + w * 576 + l * 8 + 4) = f32x4{y[4], y[5], y[6], y[7]};
    if ((l & 8) == 0) *(LAS f32x2*)(sy + w * 576 + 512 + (l & 7) * 8 + (l >> 5) * 4 + ((l >> 4) & 1) * 2) = z2;
    WAITL0(); BAR();
    STAMP(15);
    if (tid < 288) {
      f32x2 v = *(LAS f32x2*)(sy + 2 * tid);
#pragma unroll
      for (int k = 1; k < 8; ++k) v += *(LAS f32x2*)(sy + k * 576 + 2 * tid);
      __builtin_amdgcn_raw_buffer_store_b128(v4u{asu(v[0]), 1u, asu(v[1]), 1u}, rsw, (tid >> 3) * 9216 + (tid & 7) * 16, WS_YG + fb * 128, AUX_SC1);
    }
  } else {
    float a0 = 0, a1 = 0;
    unsigned base_out = 0;
    if (w == 0) {
      for (unsigned sp = 0; sp < SPIN_CAP; ++sp)
        if (__hip_atomic_load((GAS u64*)(p.ws + WS_F3) + (wrow >> 4), __ATOMIC_RELAXED, __HIP_MEMORY_SCOPE_AGENT) == E) break;
      asm volatile("" ::: "memory");
      if (tid < 8) base_out = __builtin_amdgcn_raw_buffer_load_b32(rso, tid * 4, wrow * 1152 + rb * 32, AUX_SC1);
    }
    if (w >= 1 && w < 4) {
      v4u g[3];
      for (unsigned sp = 0; sp < SPIN_CAP; ++sp) {
        asm volatile("" ::: "memory");
#pragma unroll
        for (int k = 0; k < 3; ++k) g[k] = __builtin_amdgcn_raw_buffer_load_b128(rsw, l * 16 + k * 1024, WS_YG + rb * 9216 + (w - 1) * 3072, AUX_SC1);
        if (__all((g[0][1] & g[0][3] & g[1][1] & g[1][3] & g[2][1] & g[2][3]) == 1u)) break;
      }
#pragma unroll
      for (int k = 0; k < 3; ++k) { const unsigned t0 = g[k][0], t1 = g[k][2]; a0 += asf(t0); a1 += asf(t1); }
      float a = red32(a0, a1); a = red16(a, a); a += dppf<DPP_R8>(a);
      if ((l & 24) == 0) sy[(w - 1) * 16 + 2 * (l & 7) + (l >> 5)] = a;
      WAITL0();
    }
    BAR();
    STAMP(16);
    if (tid < 8) {
      const f32x2 y = *(LAS f32x2*)(sy + 2 * tid) + *(LAS f32x2*)(sy + 16 + 2 * tid) + *(LAS f32x2*)(sy + 32 + 2 * tid);
      const float sg = __builtin_amdgcn_rcpf(1.f + __expf(-unkey(swin[0])));
      __builtin_amdgcn_raw_buffer_store_b32(pack2(bflo(base_out) + y[0] * sg, bfhi(base_out) + y[1] * sg), rso, tid * 4, wrow * 1152 + rb * 32, AUX_SC1);
      if (rb == 0 && tid == 0) __hip_atomic_store((GAS u64*)(p.ws + WS_EPOCH), E, __ATOMIC_RELAXED, __HIP_MEMORY_SCOPE_AGENT);
    }
  }
  STAMP(17);
  STAMP_FLUSH();
}

extern "C" void kernel_launch(void* const* d_in, const int* in_sizes, int n_in, void* d_out, int out_size, void* d_ws,
                              size_t ws_size, hipStream_t stream) {
  Params p;
  p.x = (const char*)d_in[0]; p.Cr = (const char*)d_in[1]; p.Ci = (const char*)d_in[2];
  p.Dr = (const char*)d_in[3]; p.Di = (const char*)d_in[4]; p.wr = (const char*)d_in[6];
  p.wg = (const char*)d_in[7]; p.wu = (const char*)d_in[8]; p.wd = (const char*)d_in[9];
  p.out = (char*)d_out; p.ws = (char*)d_ws;
  (void)hipFuncSetAttribute((const void*)fused, hipFuncAttributeMaxDynamicSharedMemorySize, LDS_BYTES);
  fused<<<256, 512, LDS_BYTES, stream>>>(p);
}
```

```cpp
#define PREREAD 31
#include <hip/hip_runtime.h>
#include <hip/hip_bf16.h>
#include <stdint.h>

#define LAS __attribute__((address_space(3)))
#define GAS __attribute__((address_space(1)))
#define CAS __attribute__((address_space(4)))
typedef short bf16x8 __attribute__((ext_vector_type(8)));
typedef __bf16 bf16x2 __attribute__((ext_vector_type(2)));
typedef float f32x4 __attribute__((ext_vector_type(4)));
typedef float f32x2 __attribute__((ext_vector_type(2)));
typedef unsigned v4u __attribute__((ext_vector_type(4)));
typedef unsigned v2u __attribute__((ext_vector_type(2)));
typedef unsigned long long u64;
typedef __amdgpu_buffer_rsrc_t rsrc_t;

constexpr int S = 2048, D = 576, F = 4608;
constexpr int AUX_SC1 = 16;
#define NPOST 0
#define PSLEEP 2
#define AUX_XD 0
#define AUX_C 0
#define AUX_W 0
constexpr int WS_EPOCH = 0;
constexpr int WS_F3 = 4096;
constexpr int WS_GRAN = 8192;
constexpr int WS_F2 = 32768;
constexpr int WS_F1 = 65536;
constexpr int WS_CAND = 131072;
constexpr int WS_YG = WS_CAND + 128 * 2304;
constexpr int WS_BF = 2 << 20;
constexpr int WS_PART = WS_BF + 256 * 18432;
constexpr int WS_END = WS_PART + 256 * 16 * 2304;
constexpr int L_BR = 0, BSLOT = 18432;
constexpr int L_AR = 4 * BSLOT, ASLOT = 16384;
constexpr int L_MISC = L_AR + 9 * 8192;
constexpr int L_DBG = L_MISC + 2048;
constexpr int L_WR = L_MISC + 4096;
constexpr int L_SC = L_WR + 2048;
constexpr int LDS_BYTES = L_SC + 8192;
constexpr int CODE_LINES = 180;
constexpr unsigned SPIN_CAP = 1u << 20;

struct Params {
  const char *x, *Cr, *Ci, *Dr, *Di, *wr, *wg, *wu, *wd;
  char* out;
  char* ws;
};

__device__ __forceinline__ float asf(unsigned u) { return __builtin_bit_cast(float, u); }
__device__ __forceinline__ unsigned asu(float f) { return __builtin_bit_cast(unsigned, f); }
__device__ __forceinline__ float bflo(unsigned b) { return asf(b << 16); }
__device__ __forceinline__ float bfhi(unsigned b) { return asf(b & 0xffff0000u); }
__device__ __forceinline__ unsigned pack2(float lo, float hi) {
  bf16x2 v; v.x = (__bf16)lo; v.y = (__bf16)hi;
  return __builtin_bit_cast(unsigned, v);
}
__device__ __forceinline__ f32x2 unp(unsigned v) { return f32x2{bflo(v), bfhi(v)}; }
__device__ __forceinline__ unsigned pk(f32x2 v) { return pack2(v[0], v[1]); }
__device__ __forceinline__ float dot2(unsigned a, unsigned b, float c) {
  return __builtin_amdgcn_fdot2_f32_bf16(__builtin_bit_cast(bf16x2, a), __builtin_bit_cast(bf16x2, b), c, false);
}
__device__ __forceinline__ f32x4 mfma(v4u a, v4u b, f32x4 c) {
  return __builtin_amdgcn_mfma_f32_16x16x32_bf16(__builtin_bit_cast(bf16x8, a), __builtin_bit_cast(bf16x8, b), c, 0, 0, 0);
}
template <int CTRL> __device__ __forceinline__ unsigned dppu(unsigned v) { return (unsigned)__builtin_amdgcn_update_dpp(0, (int)v, CTRL, 0xf, 0xf, true); }
template <int CTRL> __device__ __forceinline__ float dppf(float v) { return asf(dppu<CTRL>(asu(v))); }
constexpr int DPP_X1 = 0xB1, DPP_X2 = 0x4E, DPP_HM = 0x141, DPP_R4 = 0x124, DPP_R8 = 0x128;
__device__ __forceinline__ float red16(float a, float b) { auto r = __builtin_amdgcn_permlane16_swap(asu(a), asu(b), false, false); return asf(r[0]) + asf(r[1]); }
__device__ __forceinline__ float red32(float a, float b) { auto r = __builtin_amdgcn_permlane32_swap(asu(a), asu(b), false, false); return asf(r[0]) + asf(r[1]); }
__device__ __forceinline__ rsrc_t mkrs(const void* p, unsigned bytes) { return __builtin_amdgcn_make_buffer_rsrc((void*)p, 0, bytes, 0x00020000); }
template <int AUX> __device__ __forceinline__ void dma(rsrc_t rs, LAS char* dst, int voff, int soff) {
  __builtin_amdgcn_raw_ptr_buffer_load_lds(rs, dst, 16, voff, soff, 0, AUX);
}
#define BAR() asm volatile("s_barrier" ::: "memory")
#define WAITV(n) asm volatile("s_waitcnt vmcnt(%0)" ::"n"(n) : "memory")
#define WAITL0() asm volatile("s_waitcnt lgkmcnt(0)" ::: "memory")
#define SCHED() __builtin_amdgcn_sched_barrier(0)
#define LDSV4(p) (*(LAS v4u*)(p))
#define SGB(m, n) __builtin_amdgcn_sched_group_barrier(m, n, 0)

#define STAMP(i)
#define STAMP_INIT()
#define STAMP_FLUSH()

__device__ __forceinline__ unsigned fkey(float r) { const unsigned u = asu(r); return (u & 0x80000000u) ? ~u : (u | 0x80000000u); }
__device__ __forceinline__ float unkey(unsigned k) { return asf((k & 0x80000000u) ? (k & 0x7fffffffu) : ~k); }
__device__ __forceinline__ unsigned umax(unsigned a, unsigned b) { return a > b ? a : b; }

__global__ __launch_bounds__(512, 2) void fused(Params p) {
  extern __shared__ __attribute__((aligned(16))) char smem[];
  LAS char* L = (LAS char*)smem;
  u64 pc0;
  asm volatile("s_getpc_b64 %0" : "=s"(pc0));
  const int b = blockIdx.x, tid = threadIdx.x, l = tid & 63, w = __builtin_amdgcn_readfirstlane(tid >> 6);
  const int xcd = b & 7, jj = b >> 3, q = xcd >> 1, mh = xcd & 1, mp = jj >> 2, n = jj & 3, m = mh * 8 + mp;
  const int sig = (2 * mp + mh + 4 * n) & 15, panel = q * 4 + n;
  const rsrc_t rsw = mkrs(p.ws, WS_END);
  u64 E;
#define LOAD_EPOCH() do { asm volatile("s_load_dwordx2 %0, %1, 0x0\n s_waitcnt lgkmcnt(0)" : "=s"(E) : "s"(p.ws) : "memory"); E += 1ull; } while (0)
  const bool is_f = jj < 9, is_p = jj >= 9 && jj < 25;
  const int fb = xcd * 9 + jj, pi = xcd * 16 + jj - 9, rb = (jj - 25) * 8 + xcd;
  asm volatile("" :: "s"(p.x), "s"(p.Dr), "s"(p.ws));
  v4u wg[9], wu[9], wd[9];
  STAMP_INIT(); STAMP(0);

  {
    const u64 t0_ = __builtin_amdgcn_s_memrealtime();
    unsigned a_ = 0;
    const int to = (b * 512 + tid) * 128;
    a_ ^= __builtin_amdgcn_raw_buffer_load_b32(mkrs(p.Cr, S * S * 2), to, 0, 0) ^ __builtin_amdgcn_raw_buffer_load_b32(mkrs(p.Ci, S * S * 2), to, 0, 0);
    a_ ^= __builtin_amdgcn_raw_buffer_load_b32(mkrs(p.wg, D * F * 2), to, 0, 0) ^ __builtin_amdgcn_raw_buffer_load_b32(mkrs(p.wu, D * F * 2), to, 0, 0) ^ __builtin_amdgcn_raw_buffer_load_b32(mkrs(p.wd, D * F * 2), to, 0, 0);
    a_ ^= __builtin_amdgcn_raw_buffer_load_b32(mkrs(p.x, S * D * 2), to, 0, 0) ^ __builtin_amdgcn_raw_buffer_load_b32(mkrs(p.Dr, D * D * 2), to, 0, 0) ^ __builtin_amdgcn_raw_buffer_load_b32(mkrs(p.Di, D * D * 2), to, 0, 0);
    a_ ^= __builtin_amdgcn_raw_buffer_load_b32(mkrs(p.out, S * D * 2), to, 0, 0) ^ __builtin_amdgcn_raw_buffer_load_b32(rsw, to, 0, AUX_SC1);
    if (tid < CODE_LINES) a_ ^= *(const GAS unsigned*)((pc0 & ~255ull) + tid * 128);
    asm volatile("" :: "v"(a_));
    while (__builtin_amdgcn_s_memrealtime() - t0_ < 2000) __builtin_amdgcn_s_sleep(1);
  }
#define LOADW_GU(dst, ptr) do { const rsrc_t r_ = mkrs(ptr, D * F * 2); \
    _Pragma("unroll") for (int k = 0; k < 8; ++k) dst[k] = __builtin_amdgcn_raw_buffer_load_b128(r_, (l >> 3) * 8 * (F * 2) + (l & 7) * 16, (144 * (w >> 1) + 64 * (w & 1) + k) * (F * 2) + fb * 128, AUX_W); \
    dst[8] = __builtin_amdgcn_raw_buffer_load_b128(r_, (l >> 3) * (F * 2) + (l & 7) * 16, (144 * (w >> 1) + 128 + 8 * (w & 1)) * (F * 2) + fb * 128, AUX_W); } while (0)
#define LOADW_G() LOADW_GU(wg, p.wg)
#define LOADW_U() LOADW_GU(wu, p.wu)
#define LOADW_D() do { const rsrc_t r_ = mkrs(p.wd, D * F * 2); \
    _Pragma("unroll") for (int k = 0; k < 8; ++k) wd[k] = __builtin_amdgcn_raw_buffer_load_b128(r_, l * 16, (fb * 64 + w * 8 + k) * 1152, AUX_W); \
    wd[8] = __builtin_amdgcn_raw_buffer_load_b128(r_, (l >> 3) * 1152 + 1024 + (l & 7) * 16, (fb * 64 + w * 8) * 1152, AUX_W); } while (0)

  if (__builtin_expect(w >= 4, 1)) {
    const int lw = w & 1;
    const int rperm = 8 * (l >> 5) + ((l >> 3) & 3), rlin = l >> 3;
    const int c0 = ((l & 7) ^ (l >> 4)) * 16, c1 = c0 ^ 64;
    int ko = mp * 128;
    if (w < 6) {
      {
        const rsrc_t rsd = mkrs((q >> 1) ? p.Di : p.Dr, D * D * 2);
        int vk[9], sk[9];
#pragma unroll
        for (int k = 0; k < 9; ++k) {
          const int P = lw * 9 + k, j = P >> 1, odd = P & 1;
          const int sc = j < 8 ? 32 * (j >> 1) + 4 * (j & 1) + 16 * odd : 128 + 8 * odd;
          sk[k] = (n * 144 + sc) * 1152;
          vk[k] = (j < 8 ? rperm : rlin) * 1152 + (odd ? c1 : c0);
        }
#define ISSUED(st) do { \
          _Pragma("unroll") for (int k = 0; k < 9; ++k) dma<AUX_XD>(rsd, L + L_BR + ((st) & 3) * BSLOT + lw * 9216 + k * 1024, vk[k], sk[k] + ko); \
          ko = ko == 1024 ? 0 : ko + 128; } while (0)
        STAMP(18);
        ISSUED(0); ISSUED(1);
        LOAD_EPOCH();
        WAITV(9); STAMP(21); BAR();
        ISSUED(2); ISSUED(3); WAITV(18); BAR();
#pragma unroll 1
        for (int j = 2; j < 7; ++j) { ISSUED(j + 2); WAITV(18); BAR(); }
        WAITV(9); BAR();
        WAITV(0); STAMP(31); BAR();
      }
      BAR();
      BAR();
      STAMP(1);
      const int pbase = WS_BF + panel * 16 * BSLOT + lw * 9216;
      GAS u64* f1 = (GAS u64*)(p.ws + WS_F1) + (panel * 2 + lw) * 256 + ((l & 31) >> 1) * 16 + (l & 1);
      unsigned have = 0;
      __builtin_amdgcn_s_sleep(20);
#define NEED(st) do { const unsigned bit_ = 3u << (2 * ((sig + (st)) & 15)); \
        for (unsigned sp_ = 0; (have & bit_) != bit_ && sp_ < SPIN_CAP; ++sp_) { \
          const u64 v_ = __hip_atomic_load(f1, __ATOMIC_RELAXED, __HIP_MEMORY_SCOPE_AGENT); \
          have = (unsigned)__builtin_amdgcn_ballot_w64(v_ == E); if ((have & bit_) != bit_) __builtin_amdgcn_s_sleep(PSLEEP); } } while (0)
#define ISSUEB(st) do { NEED(st); const int so_ = pbase + ((sig + (st)) & 15) * BSLOT; \
        _Pragma("unroll") for (int k = 0; k < 9; ++k) dma<AUX_SC1>(rsw, L + L_BR + ((st) & 3) * BSLOT + lw * 9216 + k * 1024, l * 16, so_ + k * 1024); } while (0)
      ISSUEB(1);
      STAMP(3);
      ISSUEB(2);
      WAITV(9); BAR();
      STAMP(4);
      ISSUEB(3);
#pragma unroll 1
      for (int j = 2; j < 14; ++j) { ISSUEB(j + 2); WAITV(18); BAR(); }
    } else {
      const rsrc_t rsx = mkrs(p.x, S * D * 2);
      {
        const int vx0 = rperm * 1152 + c0, vx1 = rperm * 1152 + c1, sx = (((q & 1) * 16 + sig) * 64 + 32 * lw) * 1152;
#define ISSUEX(st) do { \
          _Pragma("unroll") for (int k = 0; k < 4; ++k) dma<AUX_XD>(rsx, L + L_AR + (st) * 8192 + lw * 4096 + k * 1024, (k & 1) ? vx1 : vx0, sx + (4 * (k >> 1) + 16 * (k & 1)) * 1152 + ko); \
          ko = ko == 1024 ? 0 : ko + 128; } while (0)
        ISSUEX(0); ISSUEX(1); ISSUEX(2); ISSUEX(3);
        LOAD_EPOCH();
        WAITV(12); BAR();
        WAITV(8); BAR();
#pragma unroll 1
        for (int j = 2; j < 7; ++j) { ISSUEX(j + 2); WAITV(8); BAR(); }
      }
      const rsrc_t rsc = mkrs((q >> 1) ? p.Ci : p.Cr, S * S * 2);
      const int v0 = (l >> 3) * 4096 + c0, v1 = v0 ^ 64;
      const int sb = (m * 128 + lw * 64) * 4096 + (q & 1) * 2048;
#define ISSUEA(st) do { const int so_ = sb + ((sig + (st)) & 15) * 128; \
        _Pragma("unroll") for (int k = 0; k < 8; ++k) dma<AUX_C>(rsc, L + L_AR + ((st) & 3) * ASLOT + lw * 8192 + k * 1024, (k & 1) ? v1 : v0, so_ + k * 8 * 4096); } while (0)
      ISSUEA(0);
      WAITV(12); BAR();
      ISSUEA(1);
      WAITV(16); BAR();
      BAR();
      WAITV(8); BAR();
      WAITV(8 * NPOST); BAR();
      ISSUEA(2);
      ISSUEA(3);
#pragma unroll 1
      for (int j = 2; j < 14; ++j) { ISSUEA(j + 2); WAITV(16); BAR(); }
    }
    if (is_p) {
      if (w < 6) {
        const rsrc_t rsx = mkrs(p.x, S * D * 2);
#pragma unroll
        for (int k = 0; k < 9; ++k) dma<0>(rsx, L + L_BR + lw * 9216 + k * 1024, l * 16, pi * BSLOT + lw * 9216 + k * 1024);
        WAITV(18); BAR(); WAITV(9); BAR();
      } else {
        dma<0>(mkrs(p.wr, D * 2), L + L_WR + lw * 1024, l * 16, lw * 1024);
        WAITV(9); BAR(); WAITV(1); BAR();
      }
      WAITV(0);
      BAR();
    } else if (is_f) {
      LOADW_G(); SCHED();
      if (w < 6) WAITV(18); else WAITV(17);
      BAR(); WAITV(9); BAR();
      BAR();
      LOADW_U(); LOADW_D(); SCHED();
    } else {
      if (w < 6) WAITV(9); else WAITV(8);
      BAR(); WAITV(0); BAR();
    }
    STAMP(5);
  } else {
    if (tid < 81) __builtin_amdgcn_raw_buffer_store_b128(v4u{0, 0, 0, 0}, rsw, tid * 16, WS_YG + b * 1296, AUX_SC1);
    unsigned warm = 0;
    if (w == 3 && l < 8 && jj * 8 + l < CODE_LINES) warm = *(const GAS unsigned*)((pc0 & ~255ull) + (jj * 8 + l) * 128);
    LOAD_EPOCH();
    const int wa = w >> 1, wn = w & 1;
    const int o0 = (l & 15) * 128 + (((l >> 4) ^ ((l & 15) >> 1)) * 16), o1 = o0 ^ 64;
    {
      f32x4 acc[2][4], acc8 = {0, 0, 0, 0}, cs0 = {0, 0, 0, 0}, cs1 = {0, 0, 0, 0}, cs8 = {0, 0, 0, 0};
      const v4u ones = {0x3f803f80u, 0x3f803f80u, 0x3f803f80u, 0x3f803f80u};
#pragma unroll
      for (int t = 0; t < 2; ++t)
#pragma unroll
        for (int j = 0; j < 4; ++j) acc[t][j] = f32x4{0, 0, 0, 0};
      f32x4 sx = {0, 0, 0, 0}, gx = {0, 0, 0, 0};
      v4u x0[2], d0[5], x1[2], d1[5];
#define RD1(xf, df, st, o) do { \
        _Pragma("unroll") for (int t = 0; t < 2; ++t) xf[t] = LDSV4(L + L_AR + (st) * 8192 + (2 * wa + (t ^ wn)) * 2048 + (o)); \
        _Pragma("unroll") for (int j = 0; j < 4; ++j) df[j] = LDSV4(L + L_BR + ((st) & 3) * BSLOT + (4 * wn + (j ^ (2 * wa))) * 2048 + (o)); \
        df[4] = LDSV4(L + L_BR + ((st) & 3) * BSLOT + 8 * 2048 + (o)); } while (0)
#define MM1(xf, df) do { \
        cs0 = mfma(ones, df[0], cs0); cs1 = mfma(ones, df[1], cs1); cs8 = mfma(ones, df[4], cs8); sx = mfma(ones, xf[0], sx); gx = mfma(xf[0], xf[0], gx); \
        _Pragma("unroll") for (int t = 0; t < 2; ++t) _Pragma("unroll") for (int j = 0; j < 4; ++j) acc[t][j] = mfma(xf[t], df[j], acc[t][j]); \
        acc8 = mfma(xf[0], df[4], acc8); } while (0)
#define ILV1() do { _Pragma("unroll") for (int z = 0; z < 7; ++z) { SGB(0x008, 1); SGB(0x100, 1); } SGB(0x008, 7); SCHED(); } while (0)
      BAR();
      STAMP(1);
      RD1(x0, d0, 0, o0); RD1(x1, d1, 0, o1); MM1(x0, d0); SCHED(); WAITL0();
#pragma unroll
      for (int st = 1; st < 9; ++st) {
        BAR();
        RD1(x0, d0, st, o0); MM1(x1, d1); ILV1();
        RD1(x1, d1, st, o1); MM1(x0, d0); ILV1();
        WAITL0();
      }
      MM1(x1, d1);
      STAMP(2);
      LAS float* smean = (LAS float*)(L + L_MISC);
      LAS float* srstd = smean + 64;
      LAS float* scs = smean + 128;
      if (((l & 15) >> 2) == (l >> 4)) {
        const float s2 = (l & 2) ? ((l & 1) ? gx[3] : gx[2]) : ((l & 1) ? gx[1] : gx[0]);
        const float mean = sx[0] * (1.f / D);
        const float var = fmaxf(s2 * (1.f / D) - mean * mean, 0.f);
        const float rs = __builtin_amdgcn_rsqf(var + 1e-5f);
        smean[w * 16 + (l & 15)] = mean;
        srstd[w * 16 + (l & 15)] = (q >> 1) ? -rs : rs;
        scs[(4 * wn + 2 * wa) * 16 + (l & 15)] = cs0[0];
        scs[(4 * wn + 2 * wa + 1) * 16 + (l & 15)] = cs1[0];
      }
      WAITL0(); BAR();
      f32x4 mean[2], rstd[2];
#pragma unroll
      for (int t = 0; t < 2; ++t) {
        mean[t] = *(LAS f32x4*)(smean + (2 * wa + (t ^ wn)) * 16 + 4 * (l >> 4));
        rstd[t] = *(LAS f32x4*)(srstd + (2 * wa + (t ^ wn)) * 16 + 4 * (l >> 4));
      }
      LAS char* ob = L + L_BR + wa * 9216 + l * 16;
      const int gb = WS_BF + (panel * 16 + sig) * BSLOT + wa * 9216;
#pragma unroll
      for (int j = 0; j < 4; ++j) {
        const int jg = 4 * wn + (j ^ (2 * wa));
        const float c = j == 0 ? cs0[0] : j == 1 ? cs1[0] : scs[jg * 16 + (l & 15)];
        const f32x4 v0 = (acc[0][j] - mean[0] * c) * rstd[0], v1 = (acc[1][j] - mean[1] * c) * rstd[1];
        const unsigned p0 = pack2(v0[0], v0[1]), p1 = pack2(v0[2], v0[3]), p2 = pack2(v1[0], v1[1]), p3 = pack2(v1[2], v1[3]);
        const v4u o = {wn ? p2 : p0, wn ? p3 : p1, wn ? p0 : p2, wn ? p1 : p3};
        __builtin_amdgcn_raw_buffer_store_b128(o, rsw, l * 16, gb + jg * 1024, AUX_SC1);
        LDSV4(ob + jg * 1024) = o;
      }
      {
        const f32x4 v = (acc8 - mean[0] * cs8[0]) * rstd[0];
        const v2u o = {pack2(v[0], v[1]), pack2(v[2], v[3])};
        __builtin_amdgcn_raw_buffer_store_b64(o, rsw, l * 16, gb + 8 * 1024 + wn * 8, AUX_SC1);
        *(LAS v2u*)(ob + 8 * 1024 + wn * 8) = o;
      }
      WAITL0(); BAR();
    }
    STAMP(3);
    const int oa0 = o0 + wa * 8192, oa1 = o1 + wa * 8192, ob = l * 16 + wn * 4096, ob8 = l * 16 + 8192;
    f32x4 acc[4][4], acc8[2];
#pragma unroll
    for (int i = 0; i < 4; ++i)
#pragma unroll
      for (int j = 0; j < 4; ++j) acc[i][j] = f32x4{0, 0, 0, 0};
    acc8[0] = acc8[1] = f32x4{0, 0, 0, 0};
    v4u c0[4], a0[5], c1[4], a1[5];
#define RD2(c, a, slot, ks, o) do { \
      _Pragma("unroll") for (int i = 0; i < 4; ++i) { a[i] = LDSV4(L + L_BR + (slot) * BSLOT + (ks) * 9216 + ob + i * 1024); c[i] = LDSV4(L + L_AR + (slot) * ASLOT + (o) + (i ^ (2 * wn)) * 2048); } \
      a[4] = LDSV4(L + L_BR + (slot) * BSLOT + (ks) * 9216 + ob8); } while (0)
#define MM2(c, a) do { \
      _Pragma("unroll") for (int i = 0; i < 4; ++i) _Pragma("unroll") for (int j = 0; j < 4; ++j) acc[i][j] = mfma(a[j], c[i], acc[i][j]); \
      acc8[0] = mfma(a[4], c[0], acc8[0]); acc8[1] = mfma(a[4], c[1], acc8[1]); } while (0)
#define ILV2() do { _Pragma("unroll") for (int z = 0; z < 9; ++z) { SGB(0x008, 1); SGB(0x100, 1); } SGB(0x008, 9); SCHED(); } while (0)
#define STEP2(slot) do { BAR(); RD2(c0, a0, slot, 0, oa0); MM2(c1, a1); ILV2(); RD2(c1, a1, slot, 1, oa1); MM2(c0, a0); ILV2(); WAITL0(); } while (0)
    RD2(c0, a0, 0, 0, oa0); RD2(c1, a1, 0, 1, oa1); MM2(c0, a0); SCHED(); WAITL0();
    WAITV(0);
    asm volatile("" :: "v"(warm));
    if (l == 0) __hip_atomic_store((GAS u64*)(p.ws + WS_F1) + ((panel * 2 + wa) * 16 + sig) * 16 + wn, E, __ATOMIC_RELAXED, __HIP_MEMORY_SCOPE_AGENT);
    STAMP(19);
    STEP2(1);
    STAMP(4);
    STEP2(2); STEP2(3);
#pragma unroll 1
    for (int it = 0; it < 3; ++it) { STEP2(0); STEP2(1); STEP2(2); STEP2(3); }
    MM2(c1, a1);
    STAMP(5);
    const int t = l & 15, quad = l >> 4;
    const int pv = (t >> 3) * (16 * 2304) + quad * 128 + (t & 7) * 16, pv8 = (t >> 3) * (16 * 2304) + 2048 + quad * 64 + (t & 7) * 8;
#pragma unroll
    for (int i = 0; i < 4; ++i) {
      const int so = WS_PART + ((m * 16 + (wa * 4 + (i ^ (2 * wn))) * 2) * 16 + panel) * 2304;
#pragma unroll
      for (int u = 0; u < 2; ++u) {
        const f32x4 y0 = acc[i][2 * u], y1 = acc[i][2 * u + 1];
        __builtin_amdgcn_raw_buffer_store_b128(v4u{pack2(y0[0], y0[1]), pack2(y0[2], y0[3]), pack2(y1[0], y1[1]), pack2(y1[2], y1[3])}, rsw, pv, so + (wn * 2 + u) * 512, AUX_SC1);
      }
      if (i < 2) __builtin_amdgcn_raw_buffer_store_b64(v2u{pack2(acc8[i][0], acc8[i][1]), pack2(acc8[i][2], acc8[i][3])}, rsw, pv8, so, AUX_SC1);
    }
    STAMP(6);
    if (is_p) BAR();
    WAITV(0);
    STAMP(7);
    if (l == 0) __hip_atomic_store((GAS u64*)(p.ws + WS_F2) + (m * 16 + panel) * 16 + w, E, __ATOMIC_RELAXED, __HIP_MEMORY_SCOPE_AGENT);
    SCHED();
    if (is_f) { BAR(); LOADW_G(); LOADW_U(); LOADW_D(); SCHED(); }
    STAMP(20);
  }
  const unsigned tag = (unsigned)E;
  const rsrc_t rso = mkrs(p.out, S * D * 2);
  if (is_p) {
    LAS float* sst = (LAS float*)(L + L_SC);
    const int row = l & 7, quad = (l >> 3) & 3, half = l >> 5, nn = w & 3, rh = w >> 2;
    const int ea = (nn * 144 + 32 * half + 8 * quad) * 2, ee = (nn * 144 + 128 + 4 * quad + 2 * half) * 2, ro = (rh * 8 + row) * 1152;
    f32x2 h[9], wv[9];
    {
      const v4u xa = LDSV4(L + L_BR + ro + ea), xc = LDSV4(L + L_BR + ro + ea + 128), wa4 = LDSV4(L + L_WR + ea), wc4 = LDSV4(L + L_WR + ea + 128);
      const unsigned xe = *(LAS unsigned*)(L + L_BR + ro + ee), we = *(LAS unsigned*)(L + L_WR + ee);
#pragma unroll
      for (int i = 0; i < 4; ++i) { h[i] = unp(xa[i]); h[4 + i] = unp(xc[i]); wv[i] = unp(wa4[i]); wv[4 + i] = unp(wc4[i]); }
      h[8] = unp(xe); wv[8] = unp(we);
    }
    float sw;
    { const f32x2 t = ((wv[0] + wv[1]) + (wv[2] + wv[3])) + ((wv[4] + wv[5]) + (wv[6] + wv[7])) + wv[8]; sw = t[0] + t[1]; }
    sw += dppf<DPP_R8>(sw); sw = red16(sw, sw); sw = red32(sw, sw);
    const int pb = WS_PART + ((pi * 2 + rh) * 16 + nn) * 2304;
    GAS u64* f2 = (GAS u64*)(p.ws + WS_F2) + ((pi >> 3) * 16 + ((l >> 1) & 3) * 4 + nn) * 16 + ((pi >> 2) & 1) * 2 + (l & 1);
    v4u pa[4], pc[4]; unsigned pe[4];
    unsigned done = 0;
    for (unsigned sp = 0; sp < SPIN_CAP; ++sp) {
      const u64 v = __hip_atomic_load(f2, __ATOMIC_RELAXED, __HIP_MEMORY_SCOPE_AGENT);
      unsigned rdy = (unsigned)__builtin_amdgcn_ballot_w64(v == E);
      rdy &= rdy >> 1;
      if (sp == SPIN_CAP - 1) rdy = 0x55u;
      asm volatile("" ::: "memory");
#pragma unroll
      for (int qq = 0; qq < 4; ++qq)
        if ((rdy & ~done) >> (2 * qq) & 1) {
          pa[qq] = __builtin_amdgcn_raw_buffer_load_b128(rsw, l * 16, pb + qq * 4 * 2304, AUX_SC1);
          pc[qq] = __builtin_amdgcn_raw_buffer_load_b128(rsw, l * 16 + 1024, pb + qq * 4 * 2304, AUX_SC1);
          pe[qq] = __builtin_amdgcn_raw_buffer_load_b32(rsw, 2048 + quad * 64 + row * 8 + half * 4, pb + qq * 4 * 2304, AUX_SC1);
        }
      done |= rdy & 0x55u;
      if (done == 0x55u) break;
      __builtin_amdgcn_s_sleep(PSLEEP);
    }
    STAMP(8);
#pragma unroll
    for (int qq = 0; qq < 4; ++qq) {
#pragma unroll
      for (int i = 0; i < 4; ++i) { h[i] += unp(pa[qq][i]); h[4 + i] += unp(pc[qq][i]); }
      h[8] += unp(pe[qq]);
    }
    STAMP(9);
    float mu, m2, dt;
    {
      const f32x2 t = ((h[0] + h[1]) + (h[2] + h[3])) + ((h[4] + h[5]) + (h[6] + h[7])) + h[8];
      mu = (t[0] + t[1]) * (1.f / 18);
      f32x2 a = {0.f, 0.f}, c = {0.f, 0.f};
#pragma unroll
      for (int i = 0; i < 9; ++i) { const f32x2 d = h[i] - mu; a += d * d; c += h[i] * wv[i]; }
      m2 = a[0] + a[1]; dt = c[0] + c[1];
    }
    {
      const float mo = dppf<DPP_R8>(mu), d = mo - mu;
      m2 = m2 + dppf<DPP_R8>(m2) + d * d * 9.f; mu = 0.5f * (mu + mo); dt += dppf<DPP_R8>(dt);
      auto r = __builtin_amdgcn_permlane16_swap(asu(mu), asu(mu), false, false);
      float e = asf(r[0]) - asf(r[1]);
      m2 = red16(m2, m2) + e * e * 18.f; mu = 0.5f * (asf(r[0]) + asf(r[1])); dt = red16(dt, dt);
      auto r2 = __builtin_amdgcn_permlane32_swap(asu(mu), asu(mu), false, false);
      e = asf(r2[0]) - asf(r2[1]);
      m2 = red32(m2, m2) + e * e * 36.f; mu = 0.5f * (asf(r2[0]) + asf(r2[1])); dt = red32(dt, dt);
    }
    if (l < 8) *(LAS f32x4*)(sst + ((rh * 8 + row) * 4 + nn) * 4) = f32x4{mu, m2, dt, sw};
    WAITL0(); BAR();
    f32x4 st[4];
#pragma unroll
    for (int i = 0; i < 4; ++i) st[i] = *(LAS f32x4*)(sst + (((l & 15) ^ (8 * rh)) * 4 + i) * 4);
    const f32x4 tot = (st[0] + st[1]) + (st[2] + st[3]);
    float mean = tot[0] * 0.25f;
    float dv = 0;
#pragma unroll
    for (int i = 0; i < 4; ++i) { const float d = st[i][0] - mean; dv += d * d; }
    float rstd = __builtin_amdgcn_rsqf((tot[1] + 144.f * dv) * (1.f / D) + 1e-5f);
    const unsigned key = fkey(rstd * (tot[2] - mean * tot[3]));
    unsigned kmax = umax(key, dppu<DPP_X1>(key)); kmax = umax(kmax, dppu<DPP_X2>(kmax)); kmax = umax(kmax, dppu<DPP_HM>(kmax)); kmax = umax(kmax, dppu<DPP_R8>(kmax));
    unsigned hit = (unsigned)__builtin_amdgcn_ballot_w64(key == kmax) & 0xffffu;
    if (rh) hit = ((hit >> 8) | (hit << 8)) & 0xffffu;
    const int brow = __builtin_ctz(hit);
    if (tid < 8) __builtin_amdgcn_raw_buffer_store_b128(v4u{kmax, tag, (unsigned)(pi * 16 + brow), tag}, rsw, tid * 2048, WS_GRAN + pi * 16, AUX_SC1);
    { const float mo = dppf<DPP_R8>(mean), rs2 = dppf<DPP_R8>(rstd); if (l & 8) { mean = mo; rstd = rs2; } }
    f32x2 hn[9];
#pragma unroll
    for (int i = 0; i < 9; ++i) hn[i] = (h[i] - mean) * rstd;
    if (rh * 8 + row == brow) {
      const int co = nn * 576 + (4 * half + quad) * 32, cbase = WS_CAND + pi * 2304;
      __builtin_amdgcn_raw_buffer_store_b128(v4u{pk(hn[0]), tag, pk(hn[1]), tag}, rsw, co, cbase, AUX_SC1);
      __builtin_amdgcn_raw_buffer_store_b128(v4u{pk(hn[2]), tag, pk(hn[3]), tag}, rsw, co + 16, cbase, AUX_SC1);
      __builtin_amdgcn_raw_buffer_store_b128(v4u{pk(hn[4]), tag, pk(hn[5]), tag}, rsw, co + 256, cbase, AUX_SC1);
      __builtin_amdgcn_raw_buffer_store_b128(v4u{pk(hn[6]), tag, pk(hn[7]), tag}, rsw, co + 272, cbase, AUX_SC1);
      __builtin_amdgcn_raw_buffer_store_b64(v2u{pk(hn[8]), tag}, rsw, nn * 576 + 512 + (2 * quad + half) * 8, cbase, AUX_SC1);
    }
    STAMP(10);
#pragma unroll
    for (int i = 0; i < 9; ++i) h[i] += hn[i];
    __builtin_amdgcn_raw_buffer_store_b128(v4u{pk(h[0]), pk(h[1]), pk(h[2]), pk(h[3])}, rso, ro + ea, pi * BSLOT, AUX_SC1);
    __builtin_amdgcn_raw_buffer_store_b128(v4u{pk(h[4]), pk(h[5]), pk(h[6]), pk(h[7])}, rso, ro + ea + 128, pi * BSLOT, AUX_SC1);
    __builtin_amdgcn_raw_buffer_store_b32(pk(h[8]), rso, ro + ee, pi * BSLOT, AUX_SC1);
    WAITV(0); BAR();
    STAMP(11);
    if (tid == 0) __hip_atomic_store((GAS u64*)(p.ws + WS_F3) + pi, E, __ATOMIC_RELAXED, __HIP_MEMORY_SCOPE_AGENT);
    STAMP_FLUSH();
    return;
  }
  if (!is_f && rb >= 36) { STAMP_FLUSH(); return; }
  LAS unsigned* swin = (LAS unsigned*)(L + L_SC);
  LAS float* sgu = (LAS float*)(L + L_SC + 1024);
  LAS float* sy = (LAS float*)(L);
  if (w == 4) {
    v4u g0, g1;
    for (unsigned sp = 0; sp < SPIN_CAP; ++sp) {
      asm volatile("" ::: "memory");
      g0 = __builtin_amdgcn_raw_buffer_load_b128(rsw, l * 16, WS_GRAN + xcd * 2048, AUX_SC1);
      g1 = __builtin_amdgcn_raw_buffer_load_b128(rsw, l * 16 + 1024, WS_GRAN + xcd * 2048, AUX_SC1);
      if (__all(g0[1] == tag && g0[3] == tag && g1[1] == tag && g1[3] == tag)) break;
      __builtin_amdgcn_s_sleep(PSLEEP);
    }
    unsigned k = g0[0], id = g0[2];
    if (g1[0] > k) { k = g1[0]; id = g1[2]; }
    unsigned km = umax(k, dppu<DPP_X1>(k)); km = umax(km, dppu<DPP_X2>(km)); km = umax(km, dppu<DPP_HM>(km)); km = umax(km, dppu<DPP_R8>(km));
    { auto r = __builtin_amdgcn_permlane16_swap(km, km, false, false); km = umax(r[0], r[1]); }
    { auto r = __builtin_amdgcn_permlane32_swap(km, km, false, false); km = umax(r[0], r[1]); }
    const int src = __builtin_ctzll(__builtin_amdgcn_ballot_w64(k == km));
    if (l == src) { swin[0] = km; swin[1] = id & 2047u; }
    WAITL0();
  }
  BAR();
  STAMP(12);
  const int wrow = __builtin_amdgcn_readfirstlane(swin[1]);
  if (is_f) {
    const int cb = WS_CAND + (wrow >> 4) * 2304 + (w >> 1) * 576;
    const int ci = 8 * (w & 1) + (l >> 3);
    v4u xa, xb; v2u xc;
    for (unsigned sp = 0; sp < SPIN_CAP; ++sp) {
      asm volatile("" ::: "memory");
      xa = __builtin_amdgcn_raw_buffer_load_b128(rsw, ci * 32, cb, AUX_SC1);
      xb = __builtin_amdgcn_raw_buffer_load_b128(rsw, ci * 32 + 16, cb, AUX_SC1);
      xc = __builtin_amdgcn_raw_buffer_load_b64(rsw, 512 + (ci >> 1) * 8, cb, AUX_SC1);
      if (__all(xa[1] == tag && xa[3] == tag && xb[1] == tag && xb[3] == tag && xc[1] == tag)) break;
    }
    const v4u xv = {xa[0], xa[2], xb[0], xb[2]};
    const unsigned x8 = (ci & 1) ? xc[0] >> 16 : xc[0] & 0xffffu;
    STAMP(13);
    float g[8], u[8];
#pragma unroll
    for (int i = 0; i < 8; ++i) g[i] = u[i] = 0.f;
#pragma unroll
    for (int k = 0; k < 9; ++k) {
      unsigned xlo, xhi;
      if (k < 8) { const unsigned t = xv[k >> 1]; xlo = (k & 1) ? t >> 16 : t & 0xffffu; xhi = (k & 1) ? t & 0xffff0000u : t << 16; }
      else { xlo = x8; xhi = x8 << 16; }
#pragma unroll
      for (int i = 0; i < 4; ++i) {
        const unsigned a = wg[k][i], c = wu[k][i];
        g[2 * i] = dot2(a, xlo, g[2 * i]); g[2 * i + 1] = dot2(a, xhi, g[2 * i + 1]);
        u[2 * i] = dot2(c, xlo, u[2 * i]); u[2 * i + 1] = dot2(c, xhi, u[2 * i + 1]);
      }
    }
    float t8[8];
#pragma unroll
    for (int i = 0; i < 8; ++i) t8[i] = red32(g[i], u[i]);
#pragma unroll
    for (int i = 0; i < 4; ++i) {
      float v = red16(t8[i], t8[i + 4]); v += dppf<DPP_R8>(v);
      if ((l & 8) == 0) sgu[((8 * (l & 7) + 4 * ((l >> 4) & 1) + i) * 2 + (l >> 5)) * 8 + w] = v;
    }
    WAITL0(); BAR();
    STAMP(14);
    const int f = 8 * w + (l >> 3);
    const f32x4 ga = *(LAS f32x4*)(sgu + f * 16) + *(LAS f32x4*)(sgu + f * 16 + 4), ua = *(LAS f32x4*)(sgu + f * 16 + 8) + *(LAS f32x4*)(sgu + f * 16 + 12);
    const float gg = (ga[0] + ga[1]) + (ga[2] + ga[3]), uu = (ua[0] + ua[1]) + (ua[2] + ua[3]);
    const float gu = gg * __builtin_amdgcn_rcpf(1.f + __expf(-gg)) * uu;
    const unsigned gb = pack2(gu, gu);
    float y[8], z[8];
#pragma unroll
    for (int i = 0; i < 8; ++i) y[i] = 0.f;
#pragma unroll
    for (int k = 0; k < 8; ++k) {
      const unsigned sv = __builtin_amdgcn_readlane(gb, k * 8);
      const unsigned lo = sv & 0xffffu, hi = sv & 0xffff0000u;
#pragma unroll
      for (int i = 0; i < 4; ++i) { const unsigned a = wd[k][i]; y[2 * i] = dot2(a, lo, y[2 * i]); y[2 * i + 1] = dot2(a, hi, y[2 * i + 1]); }
    }
    const float gf = bfhi(gb);
#pragma unroll
    for (int i = 0; i < 4; ++i) { const unsigned a = wd[8][i]; z[2 * i] = bflo(a) * gf; z[2 * i + 1] = bfhi(a) * gf; }
    float z4[4]; f32x2 z2;
#pragma unroll
    for (int i = 0; i < 4; ++i) z4[i] = red32(z[i], z[i + 4]);
#pragma unroll
    for (int i = 0; i < 2; ++i) { float v = red16(z4[i], z4[i + 2]); v += dppf<DPP_R8>(v); z2[i] = v; }
    *(LAS f32x4*)(sy + w * 576 + l * 8) = f32x4{y[0], y[1], y[2], y[3]};
    *(LAS f32x4*)(sy + w * 576 + l * 8 + 4) = f32x4{y[4], y[5], y[6], y[7]};
    if ((l & 8) == 0) *(LAS f32x2*)(sy + w * 576 + 512 + (l & 7) * 8 + (l >> 5) * 4 + ((l >> 4) & 1) * 2) = z2;
    WAITL0(); BAR();
    STAMP(15);
    if (tid < 288) {
      f32x2 v = *(LAS f32x2*)(sy + 2 * tid);
#pragma unroll
      for (int k = 1; k < 8; ++k) v += *(LAS f32x2*)(sy + k * 576 + 2 * tid);
      __builtin_amdgcn_raw_buffer_store_b128(v4u{asu(v[0]), 1u, asu(v[1]), 1u}, rsw, (tid >> 3) * 9216 + (tid & 7) * 16, WS_YG + fb * 128, AUX_SC1);
    }
  } else {
    float a0 = 0, a1 = 0;
    unsigned base_out = 0;
    if (w == 0) {
      for (unsigned sp = 0; sp < SPIN_CAP; ++sp)
        if (__hip_atomic_load((GAS u64*)(p.ws + WS_F3) + (wrow >> 4), __ATOMIC_RELAXED, __HIP_MEMORY_SCOPE_AGENT) == E) break;
      asm volatile("" ::: "memory");
      if (tid < 8) base_out = __builtin_amdgcn_raw_buffer_load_b32(rso, tid * 4, wrow * 1152 + rb * 32, AUX_SC1);
    }
    if (w >= 1 && w < 4) {
      v4u g[3];
      for (unsigned sp = 0; sp < SPIN_CAP; ++sp) {
        asm volatile("" ::: "memory");
#pragma unroll
        for (int k = 0; k < 3; ++k) g[k] = __builtin_amdgcn_raw_buffer_load_b128(rsw, l * 16 + k * 1024, WS_YG + rb * 9216 + (w - 1) * 3072, AUX_SC1);
        if (__all((g[0][1] & g[0][3] & g[1][1] & g[1][3] & g[2][1] & g[2][3]) == 1u)) break;
      }
#pragma unroll
      for (int k = 0; k < 3; ++k) { const unsigned t0 = g[k][0], t1 = g[k][2]; a0 += asf(t0); a1 += asf(t1); }
      float a = red32(a0, a1); a = red16(a, a); a += dppf<DPP_R8>(a);
      if ((l & 24) == 0) sy[(w - 1) * 16 + 2 * (l & 7) + (l >> 5)] = a;
      WAITL0();
    }
    BAR();
    STAMP(16);
    if (tid < 8) {
      const f32x2 y = *(LAS f32x2*)(sy + 2 * tid) + *(LAS f32x2*)(sy + 16 + 2 * tid) + *(LAS f32x2*)(sy + 32 + 2 * tid);
      const float sg = __builtin_amdgcn_rcpf(1.f + __expf(-unkey(swin[0])));
      __builtin_amdgcn_raw_buffer_store_b32(pack2(bflo(base_out) + y[0] * sg, bfhi(base_out) + y[1] * sg), rso, tid * 4, wrow * 1152 + rb * 32, AUX_SC1);
      if (rb == 0 && tid == 0) __hip_atomic_store((GAS u64*)(p.ws + WS_EPOCH), E, __ATOMIC_RELAXED, __HIP_MEMORY_SCOPE_AGENT);
    }
  }
  STAMP(17);
  STAMP_FLUSH();
}

extern "C" void kernel_launch(void* const* d_in, const int* in_sizes, int n_in, void* d_out, int out_size, void* d_ws,
                              size_t ws_size, hipStream_t stream) {
  Params p;
  p.x = (const char*)d_in[0]; p.Cr = (const char*)d_in[1]; p.Ci = (const char*)d_in[2];
  p.Dr = (const char*)d_in[3]; p.Di = (const char*)d_in[4]; p.wr = (const char*)d_in[6];
  p.wg = (const char*)d_in[7]; p.wu = (const char*)d_in[8]; p.wd = (const char*)d_in[9];
  p.out = (char*)d_out; p.ws = (char*)d_ws;
  (void)hipFuncSetAttribute((const void*)fused, hipFuncAttributeMaxDynamicSharedMemorySize, LDS_BYTES);
  fused<<<256, 512, LDS_BYTES, stream>>>(p);
}
```

```cpp
#include <hip/hip_runtime.h>
#include <hip/hip_bf16.h>
#include <stdint.h>

#define LAS __attribute__((address_space(3)))
#define GAS __attribute__((address_space(1)))
#define CAS __attribute__((address_space(4)))
typedef short bf16x8 __attribute__((ext_vector_type(8)));
typedef __bf16 bf16x2 __attribute__((ext_vector_type(2)));
typedef float f32x4 __attribute__((ext_vector_type(4)));
typedef float f32x2 __attribute__((ext_vector_type(2)));
typedef unsigned v4u __attribute__((ext_vector_type(4)));
typedef unsigned v2u __attribute__((ext_vector_type(2)));
typedef unsigned long long u64;
typedef __amdgpu_buffer_rsrc_t rsrc_t;

constexpr int S = 2048, D = 576, F = 4608;
constexpr int AUX_SC1 = 16;
#define TOUCH1 0
#define TOUCH2 1
#define GEARLY 0
#define FSLEEP 1
#define RSLEEP 36
#define OUTMODE 0
#define OUTSLEEP 40
#define TOUCH3 1
#define NPOST 2
#define SLEEP1 12
#define PSLEEP 2
#define AUX_XD 0
#define AUX_C 0
#define AUX_W 0
constexpr int WS_EPOCH = 0;
constexpr int WS_F3 = 4096;
constexpr int WS_GRAN = 1 << 20;
constexpr int WS_F2 = 32768;
constexpr int WS_F1 = 98304;
constexpr int WS_CAND = 163840;
constexpr int WS_YG = WS_CAND + 128 * 2304;
constexpr int WS_BF = 2 << 20;
constexpr int WS_PART = WS_BF + 256 * 18432;
constexpr int WS_END = WS_PART + 256 * 16 * 2304;
constexpr int L_BR = 0, BSLOT = 18432;
constexpr int L_AR = 4 * BSLOT, ASLOT = 16384;
constexpr int L_MISC = L_AR + 9 * 8192;
constexpr int L_DBG = L_MISC + 2048;
constexpr int L_WR = L_MISC + 4096;
constexpr int L_SC = L_WR + 2048;
constexpr int LDS_BYTES = L_SC + 8192;
constexpr int CODE_LINES = 180;
constexpr unsigned SPIN_CAP = 1u << 16;

struct Params {
  const char *x, *Cr, *Ci, *Dr, *Di, *wr, *wg, *wu, *wd;
  char* out;
  char* ws;
};

__device__ __forceinline__ float asf(unsigned u) { return __builtin_bit_cast(float, u); }
__device__ __forceinline__ unsigned asu(float f) { return __builtin_bit_cast(unsigned, f); }
__device__ __forceinline__ float bflo(unsigned b) { return asf(b << 16); }
__device__ __forceinline__ float bfhi(unsigned b) { return asf(b & 0xffff0000u); }
__device__ __forceinline__ unsigned pack2(float lo, float hi) {
  bf16x2 v; v.x = (__bf16)lo; v.y = (__bf16)hi;
  return __builtin_bit_cast(unsigned, v);
}
__device__ __forceinline__ f32x2 unp(unsigned v) { return f32x2{bflo(v), bfhi(v)}; }
__device__ __forceinline__ unsigned pk(f32x2 v) { return pack2(v[0], v[1]); }
__device__ __forceinline__ float dot2(unsigned a, unsigned b, float c) {
  return __builtin_amdgcn_fdot2_f32_bf16(__builtin_bit_cast(bf16x2, a), __builtin_bit_cast(bf16x2, b), c, false);
}
__device__ __forceinline__ f32x4 mfma(v4u a, v4u b, f32x4 c) {
  return __builtin_amdgcn_mfma_f32_16x16x32_bf16(__builtin_bit_cast(bf16x8, a), __builtin_bit_cast(bf16x8, b), c, 0, 0, 0);
}
template <int CTRL> __device__ __forceinline__ unsigned dppu(unsigned v) { return (unsigned)__builtin_amdgcn_update_dpp(0, (int)v, CTRL, 0xf, 0xf, true); }
template <int CTRL> __device__ __forceinline__ float dppf(float v) { return asf(dppu<CTRL>(asu(v))); }
constexpr int DPP_X1 = 0xB1, DPP_X2 = 0x4E, DPP_HM = 0x141, DPP_R4 = 0x124, DPP_R8 = 0x128;
__device__ __forceinline__ float red16(float a, float b) { auto r = __builtin_amdgcn_permlane16_swap(asu(a), asu(b), false, false); return asf(r[0]) + asf(r[1]); }
__device__ __forceinline__ float red32(float a, float b) { auto r = __builtin_amdgcn_permlane32_swap(asu(a), asu(b), false, false); return asf(r[0]) + asf(r[1]); }
__device__ __forceinline__ rsrc_t mkrs(const void* p, unsigned bytes) { return __builtin_amdgcn_make_buffer_rsrc((void*)p, 0, bytes, 0x00020000); }
template <int AUX> __device__ __forceinline__ void dma(rsrc_t rs, LAS char* dst, int voff, int soff) {
  __builtin_amdgcn_raw_ptr_buffer_load_lds(rs, dst, 16, voff, soff, 0, AUX);
}
__device__ __forceinline__ void touch(rsrc_t rs, LAS char* L, int voff) { __builtin_amdgcn_raw_ptr_buffer_load_lds(rs, L + L_MISC + 1792, 4, voff, 0, 0, 0); }
#define BAR() asm volatile("s_barrier" ::: "memory")
#define WAITV(n) asm volatile("s_waitcnt vmcnt(%0)" ::"n"(n) : "memory")
#define WAITL0() asm volatile("s_waitcnt lgkmcnt(0)" ::: "memory")
#define SCHED() __builtin_amdgcn_sched_barrier(0)
#define LDSV4(p) (*(LAS v4u*)(p))
#define SGB(m, n) __builtin_amdgcn_sched_group_barrier(m, n, 0)

#define STAMP(i)
#define STAMPC(i)
#define STAMP_INIT()
#define STAMP_FLUSH()

__device__ __forceinline__ unsigned fkey(float r) { const unsigned u = asu(r); return (u & 0x80000000u) ? ~u : (u | 0x80000000u); }
__device__ __forceinline__ float unkey(unsigned k) { return asf((k & 0x80000000u) ? (k & 0x7fffffffu) : ~k); }
__device__ __forceinline__ unsigned umax(unsigned a, unsigned b) { return a > b ? a : b; }

__global__ __launch_bounds__(512, 2) void fused(Params p) {
  extern __shared__ __attribute__((aligned(16))) char smem[];
  LAS char* L = (LAS char*)smem;
  u64 pc0;
  asm volatile("s_getpc_b64 %0" : "=s"(pc0));
  const int b = blockIdx.x, tid = threadIdx.x, l = tid & 63, w = __builtin_amdgcn_readfirstlane(tid >> 6);
  const int xcd = b & 7, jj = b >> 3, q = xcd >> 1, mh = xcd & 1, mp = jj >> 2, n = jj & 3, m = mh * 8 + mp;
  const int sig = (2 * mp + mh + 4 * n) & 15, panel = q * 4 + n;
  const rsrc_t rsw = mkrs(p.ws, WS_END);
  u64 E;
#define KIDX(st) ((sig + 2 * ((st) & 7) + ((st) >> 3)) & 15)
#define LOAD_EPOCH() do { asm volatile("s_load_dwordx2 %0, %1, 0x0\n s_waitcnt lgkmcnt(0)" : "=s"(E) : "s"(p.ws) : "memory"); E += 1ull; } while (0)
  const bool is_f = jj < 9, is_p = jj >= 9 && jj < 25;
  const int fb = xcd * 9 + jj, pi = xcd * 16 + jj - 9, rb = (jj - 25) * 8 + xcd;
  asm volatile("" :: "s"(p.x), "s"(p.Dr), "s"(p.ws));
  v4u wg[9], wu[9], wd[10];
  STAMP_INIT(); STAMP(0);

#define WSZ (D * F * 2)
#define LOADW_GU(dst, ptr) do { const rsrc_t r_ = mkrs(ptr, WSZ); \
    _Pragma("unroll") for (int k = 0; k < 8; ++k) dst[k] = __builtin_amdgcn_raw_buffer_load_b128(r_, (l >> 3) * 8 * (F * 2) + (l & 7) * 16, (144 * (w >> 1) + 64 * (w & 1) + k) * (F * 2) + fb * 128, AUX_W); \
    dst[8] = __builtin_amdgcn_raw_buffer_load_b128(r_, (l >> 3) * (F * 2) + (l & 7) * 16, (144 * (w >> 1) + 128 + 8 * (w & 1)) * (F * 2) + fb * 128, AUX_W); } while (0)
#define TOUCHW3() do { const int li_ = ((jj < 9 ? jj : jj - 16) * 8 + w) * 61 + l, lu_ = li_ - 5184; \
    touch(mkrs(p.wg, WSZ), L, l < 61 && li_ < 5184 ? (li_ / 9) * (F * 2) + (xcd * 9 + li_ % 9) * 128 : -1); \
    touch(mkrs(p.wu, WSZ), L, l < 61 && lu_ >= 0 && lu_ < 2592 ? (lu_ / 9) * (F * 2) + (xcd * 9 + lu_ % 9) * 128 : -1); \
    if (TOUCH3 == 2) { touch(mkrs(p.wd, WSZ), L, l < 61 && li_ < 5184 ? (xcd * 5184 + li_) * 128 : -1); \
      touch(mkrs(p.wu, WSZ), L, l < 61 && lu_ >= 0 && lu_ < 2592 ? (288 + lu_ / 9) * (F * 2) + (xcd * 9 + lu_ % 9) * 128 : -1); } } while (0)
#define LOADW_G() LOADW_GU(wg, p.wg)
#define LOADW_U() LOADW_GU(wu, p.wu)
#define LOADW_D() do { const rsrc_t r_ = mkrs(p.wd, WSZ); \
    _Pragma("unroll") for (int k = 0; k < 8; ++k) wd[k] = __builtin_amdgcn_raw_buffer_load_b128(r_, (l >> 4) * 2304 + (l & 15) * 16, (fb * 64 + w * 8 + (k & 1)) * 1152 + (k >> 1) * 256, AUX_W); \
    _Pragma("unroll") for (int k = 8; k < 10; ++k) wd[k] = __builtin_amdgcn_raw_buffer_load_b128(r_, (l >> 4) * 2304 + (l & 7) * 16, (fb * 64 + w * 8 + (k & 1)) * 1152 + 1024, AUX_W); } while (0)

  if (__builtin_expect(w >= 4, 1)) {
    const int lw = w & 1;
    const int rperm = 8 * (l >> 5) + ((l >> 3) & 3), rlin = l >> 3;
    const int c0 = ((l & 7) ^ (l >> 4)) * 16, c1 = c0 ^ 64;
    int ko = mp * 128;
    if (w < 6) {
      {
        const rsrc_t rsd = mkrs((q >> 1) ? p.Di : p.Dr, D * D * 2);
        const int cw = lw ? c1 : c0, vp = rperm * 1152 + cw, vl = rlin * 1152 + cw, sd = n * 144 * 1152;
#define ISSUED(st) do { \
          _Pragma("unroll") for (int k = 0; k < 8; ++k) dma<AUX_XD>(rsd, L + L_BR + ((st) & 3) * BSLOT + lw * 1024 + k * 2048, vp, sd + lw * (16 * 1152) + (32 * (k >> 1) + 4 * (k & 1)) * 1152 + ko); \
          dma<AUX_XD>(rsd, L + L_BR + ((st) & 3) * BSLOT + lw * 1024 + 8 * 2048, vl, sd + lw * (8 * 1152) + 128 * 1152 + ko); \
          ko = ko == 1024 ? 0 : ko + 128; } while (0)
        STAMP(18);
        ISSUED(0); ISSUED(1);
        LOAD_EPOCH();
        WAITV(9); STAMP(21); BAR();
        ISSUED(2); ISSUED(3); WAITV(18); BAR();
#pragma unroll 1
        for (int j = 2; j < 7; ++j) { ISSUED(j + 2); WAITV(18); BAR(); }
        WAITV(9); BAR();
        WAITV(0); STAMP(31); BAR();
      }
      BAR();
      BAR();
      STAMP(1);
      const int pbase = WS_BF + panel * 16 * BSLOT + lw * 9216;
      GAS u64* f1 = (GAS u64*)(p.ws + WS_F1) + (panel * 2 + lw) * 256;
      unsigned have = 0;
      __builtin_amdgcn_s_sleep(SLEEP1);
      for (unsigned sp = 0; sp < SPIN_CAP; ++sp)
        if (__hip_atomic_load(f1 + KIDX(1) * 16, __ATOMIC_RELAXED, __HIP_MEMORY_SCOPE_AGENT) == E) break;
      have = 1u << KIDX(1);
#define NEED(st) do { const unsigned bit_ = 1u << KIDX(st); \
        for (unsigned sp_ = 0; !(have & bit_) && sp_ < SPIN_CAP; ++sp_) \
          have = (unsigned)__builtin_amdgcn_ballot_w64(__hip_atomic_load(f1 + (l & 15) * 16, __ATOMIC_RELAXED, __HIP_MEMORY_SCOPE_AGENT) == E); } while (0)
#define ISSUEB(st) do { NEED(st); const int so_ = pbase + KIDX(st) * BSLOT; \
        _Pragma("unroll") for (int k = 0; k < 9; ++k) dma<AUX_SC1>(rsw, L + L_BR + ((st) & 3) * BSLOT + lw * 9216 + k * 1024, l * 16, so_ + k * 1024); } while (0)
      {
        u64 va;
        asm volatile("global_load_dwordx2 %0, %1, off sc1" : "=v"(va) : "v"(f1 + (l & 15) * 16) : "memory");
        ISSUEB(1);
        asm volatile("s_waitcnt vmcnt(9)" : "+v"(va) :: "memory");
        have |= (unsigned)__builtin_amdgcn_ballot_w64(va == E);
      }
      STAMP(3);
      ISSUEB(2);
      WAITV(9); BAR();
      STAMP(4);
      ISSUEB(3);
#pragma unroll 1
      for (int j = 2; j < 14; ++j) { ISSUEB(j + 2); WAITV(18); BAR(); }
    } else {
      const rsrc_t rsx = mkrs(p.x, S * D * 2);
      {
        const int vx0 = rperm * 1152 + c0, vx1 = rperm * 1152 + c1, sx = (((q & 1) * 16 + sig) * 64 + 32 * lw) * 1152;
#define ISSUEX(st) do { \
          _Pragma("unroll") for (int k = 0; k < 4; ++k) dma<AUX_XD>(rsx, L + L_AR + (st) * 8192 + lw * 4096 + k * 1024, (k & 1) ? vx1 : vx0, sx + (4 * (k >> 1) + 16 * (k & 1)) * 1152 + ko); \
          ko = ko == 1024 ? 0 : ko + 128; } while (0)
        ISSUEX(0); ISSUEX(1); ISSUEX(2); ISSUEX(3);
        LOAD_EPOCH();
        WAITV(12); BAR();
        WAITV(8); BAR();
#pragma unroll 1
        for (int j = 2; j < 7; ++j) { ISSUEX(j + 2); WAITV(8); BAR(); }
      }
      const rsrc_t rsc = mkrs((q >> 1) ? p.Ci : p.Cr, S * S * 2);
      const int v0 = (l >> 3) * 4096 + c0, v1 = v0 ^ 64;
      const int sb = (m * 128 + lw * 64) * 4096 + (q & 1) * 2048;
#define ISSUEA(st) do { const int so_ = sb + KIDX(st) * 128; \
        _Pragma("unroll") for (int k = 0; k < 8; ++k) dma<AUX_C>(rsc, L + L_AR + ((st) & 3) * ASLOT + lw * 8192 + k * 1024, (k & 1) ? v1 : v0, so_ + k * 8 * 4096); } while (0)
      ISSUEA(0);
      WAITV(12); BAR();
      ISSUEA(1);
      WAITV(16); BAR();
      BAR();
      WAITV(8); BAR();
      ISSUEA(2);
      ISSUEA(3);
      WAITV(8 * NPOST); BAR();
#pragma unroll 1
      for (int j = 2; j < 14; ++j) { ISSUEA(j + 2); WAITV(16); BAR(); }
      if (lw == 0) { touch(mkrs(p.wd, WSZ), L, ((xcd * 32 + jj) * 162 + l) * 128); touch(mkrs(p.wd, WSZ), L, ((xcd * 32 + jj) * 162 + 64 + l) * 128); touch(mkrs(p.wd, WSZ), L, -1); }
      else { touch(mkrs(p.wd, WSZ), L, l < 34 ? ((xcd * 32 + jj) * 162 + 128 + l) * 128 : -1); touch(mkrs(p.wu, WSZ), L, l < 63 ? (288 + jj * 9 + l / 9) * (F * 2) + (xcd * 9 + l % 9) * 128 : -1);
             touch(mkrs(p.wu, WSZ), L, l < 18 ? (288 + jj * 9 + 7 + l / 9) * (F * 2) + (xcd * 9 + l % 9) * 128 : -1); }
    }
    if (is_p) {
      if (w < 6) {
        const rsrc_t rsx = mkrs(p.x, S * D * 2);
#pragma unroll
        for (int k = 0; k < 9; ++k) dma<0>(rsx, L + L_BR + lw * 9216 + k * 1024, l * 16, pi * BSLOT + lw * 9216 + k * 1024);
        WAITV(18); BAR(); WAITV(9); BAR();
      } else {
        dma<0>(mkrs(p.wr, D * 2), L + L_WR + lw * 1024, l * 16, lw * 1024);
        WAITV(9 + 3 * TOUCH2); BAR(); WAITV(1 + 3 * TOUCH2); BAR();
      }
      WAITV(0);
      BAR();
    } else if (is_f) {
      if (w < 6) { WAITV(9); BAR(); WAITV(0); } else { WAITV(8 + 3 * TOUCH2); BAR(); WAITV(3 * TOUCH2); }
      BAR();
      BAR();
      TOUCHW3(); LOADW_D(); SCHED(); LOADW_U(); SCHED(); LOADW_G(); SCHED();
    } else {
      if (w < 6) { WAITV(9); BAR(); WAITV(0); } else { WAITV(8 + 3 * TOUCH2); BAR(); WAITV(3 * TOUCH2); }
      BAR();
      BAR(); TOUCHW3();
    }
    STAMP(5);
  } else {
    if (tid < 81) __builtin_amdgcn_raw_buffer_store_b128(v4u{0, 0, 0, 0}, rsw, tid * 16, WS_YG + b * 1296, AUX_SC1);
    LAS unsigned* cnt = (LAS unsigned*)(L + L_MISC + 1536);
    if (tid < 8) cnt[tid] = 0u;
    unsigned warm = 0;
    if (w == 3 && l < 8 && jj * 8 + l < CODE_LINES) warm = *(const GAS unsigned*)((pc0 & ~255ull) + (jj * 8 + l) * 128);
    LOAD_EPOCH();
    const int wa = w >> 1, wn = w & 1;
    const int o0 = (l & 15) * 128 + (((l >> 4) ^ ((l & 15) >> 1)) * 16), o1 = o0 ^ 64;
    {
      f32x4 acc[2][4], acc8 = {0, 0, 0, 0}, cs0 = {0, 0, 0, 0}, cs1 = {0, 0, 0, 0}, cs8 = {0, 0, 0, 0};
      const v4u ones = {0x3f803f80u, 0x3f803f80u, 0x3f803f80u, 0x3f803f80u};
#pragma unroll
      for (int t = 0; t < 2; ++t)
#pragma unroll
        for (int j = 0; j < 4; ++j) acc[t][j] = f32x4{0, 0, 0, 0};
      f32x4 sx = {0, 0, 0, 0}, gx = {0, 0, 0, 0};
      v4u x0[2], d0[5], x1[2], d1[5];
#define RD1(xf, df, st, o) do { \
        _Pragma("unroll") for (int t = 0; t < 2; ++t) xf[t] = LDSV4(L + L_AR + (st) * 8192 + (2 * wa + (t ^ wn)) * 2048 + (o)); \
        _Pragma("unroll") for (int j = 0; j < 4; ++j) df[j] = LDSV4(L + L_BR + ((st) & 3) * BSLOT + (4 * wn + (j ^ (2 * wa))) * 2048 + (o)); \
        df[4] = LDSV4(L + L_BR + ((st) & 3) * BSLOT + 8 * 2048 + (o)); } while (0)
#define MS1(xf, df) do { sx = mfma(ones, xf[0], sx); gx = mfma(xf[0], xf[0], gx); cs0 = mfma(ones, df[0], cs0); cs1 = mfma(ones, df[1], cs1); cs8 = mfma(ones, df[4], cs8); } while (0)
#define MA1(xf, df) do { \
        _Pragma("unroll") for (int j = 0; j < 4; ++j) _Pragma("unroll") for (int t = 0; t < 2; ++t) acc[t][j] = mfma(xf[t], df[j], acc[t][j]); \
        acc8 = mfma(xf[0], df[4], acc8); } while (0)
#define MM1(xf, df) do { MS1(xf, df); MA1(xf, df); } while (0)
#define ILV1() do { _Pragma("unroll") for (int z = 0; z < 7; ++z) { SGB(0x008, 1); SGB(0x100, 1); } SGB(0x008, 7); SCHED(); } while (0)
      int a_st = L_MISC + (w * 16 + (l & 15)) * 4, a_cs = L_MISC + 512 + ((4 * wn + 2 * wa) * 16 + (l & 15)) * 4;
      int a_m0 = L_MISC + (w * 16 + 4 * (l >> 4)) * 4, a_ob = L_BR + wa * 9216 + (4 * wn + 2 * wa) * 1024 + l * 16, a_o8 = L_BR + wa * 9216 + 8192 + l * 16 + wn * 8;
      int g_off = l * 16 + (4 * wn + 2 * wa) * 1024, g_o8 = l * 16 + 8192 + wn * 8;
      const bool diag = ((l & 15) >> 2) == (l >> 4), b0 = l & 1, b1 = l & 2;
      const float sgn = (q >> 1) ? -1.f : 1.f;
      const int gb = WS_BF + (panel * 16 + sig) * BSLOT + wa * 9216;
      asm volatile("" : "+v"(a_st), "+v"(a_cs), "+v"(a_m0), "+v"(a_ob), "+v"(a_o8), "+v"(g_off), "+v"(g_o8));
      BAR();
      STAMP(1);
      STAMPC(30);
      RD1(x0, d0, 0, o0); RD1(x1, d1, 0, o1); MM1(x0, d0); SCHED(); WAITL0();
#pragma unroll
      for (int st = 1; st < 8; ++st) {
        BAR();
        RD1(x0, d0, st, o0); MM1(x1, d1); ILV1();
        RD1(x1, d1, st, o1); MM1(x0, d0); ILV1();
        WAITL0();
      }
      BAR();
      RD1(x0, d0, 8, o0); MM1(x1, d1); ILV1();
      RD1(x1, d1, 8, o1); MS1(x0, d0); SCHED();
      MS1(x1, d1); SCHED();
      STAMP(2); STAMPC(31);
      MA1(x0, d0);
      {
        const float s2 = b1 ? (b0 ? gx[3] : gx[2]) : (b0 ? gx[1] : gx[0]);
        const float mean = sx[0] * (1.f / D);
        const float rs = sgn * __builtin_amdgcn_rsqf(fmaxf(s2 * (1.f / D) - mean * mean, 0.f) + 1e-5f);
        if (diag) {
          *(LAS float*)(L + a_st) = mean; *(LAS float*)(L + a_st + 256) = rs;
          *(LAS float*)(L + a_cs) = cs0[0]; *(LAS float*)(L + a_cs + 64) = cs1[0];
        }
      }
      WAITL0(); BAR();
      const f32x4 mean0 = *(LAS f32x4*)(L + a_m0), rstd0 = *(LAS f32x4*)(L + a_m0 + 256), mean1 = *(LAS f32x4*)(L + (a_m0 ^ 64)), rstd1 = *(LAS f32x4*)(L + (a_m0 ^ 64) + 256);
      const float c2 = *(LAS float*)(L + (a_cs ^ 128)), c3 = *(LAS float*)(L + (a_cs ^ 128) + 64);
      SCHED();
      MA1(x1, d1);
      SCHED();
      const float cj[4] = {cs0[0], cs1[0], c2, c3};
      unsigned pk0[4][2], pk1[4][2];
#pragma unroll
      for (int j = 0; j < 4; ++j) {
        const f32x4 v0 = (acc[0][j] - mean0 * cj[j]) * rstd0, v1 = (acc[1][j] - mean1 * cj[j]) * rstd1;
        pk0[j][0] = pack2(v0[0], v0[1]); pk0[j][1] = pack2(v0[2], v0[3]); pk1[j][0] = pack2(v1[0], v1[1]); pk1[j][1] = pack2(v1[2], v1[3]);
      }
      const f32x4 v8 = (acc8 - mean0 * cs8[0]) * rstd0;
      const v2u o8 = {pack2(v8[0], v8[1]), pack2(v8[2], v8[3])};
#define PUT1(A, B) do { _Pragma("unroll") for (int j = 0; j < 4; ++j) \
          __builtin_amdgcn_raw_buffer_store_b128(v4u{A[j][0], A[j][1], B[j][0], B[j][1]}, rsw, (g_off ^ ((j >> 1) * 2048)) + (j & 1) * 1024, gb, AUX_SC1); \
        __builtin_amdgcn_raw_buffer_store_b64(o8, rsw, g_o8, gb, AUX_SC1); \
        _Pragma("unroll") for (int j = 0; j < 4; ++j) LDSV4(L + a_ob + ((j >> 1) ? (wa ? -2048 : 2048) : 0) + (j & 1) * 1024) = v4u{A[j][0], A[j][1], B[j][0], B[j][1]}; } while (0)
      if (wn) PUT1(pk1, pk0); else PUT1(pk0, pk1);
      *(LAS v2u*)(L + a_o8) = o8;
      WAITL0(); BAR();
    }
    STAMP(3);
    const int oa0 = o0 + wa * 8192, oa1 = o1 + wa * 8192, ob = l * 16 + wn * 4096, ob8 = l * 16 + 8192;
    f32x4 acc[4][4], acc8[2];
#pragma unroll
    for (int i = 0; i < 4; ++i)
#pragma unroll
      for (int j = 0; j < 4; ++j) acc[i][j] = f32x4{0, 0, 0, 0};
    acc8[0] = acc8[1] = f32x4{0, 0, 0, 0};
    v4u c0[4], a0[5], c1[4], a1[5];
#define RD2(c, a, slot, ks, o) do { \
      _Pragma("unroll") for (int i = 0; i < 4; ++i) { a[i] = LDSV4(L + L_BR + (slot) * BSLOT + (ks) * 9216 + ob + i * 1024); c[i] = LDSV4(L + L_AR + (slot) * ASLOT + (o) + (i ^ (2 * wn)) * 2048); } \
      a[4] = LDSV4(L + L_BR + (slot) * BSLOT + (ks) * 9216 + ob8); } while (0)
#define MM2(c, a) do { \
      _Pragma("unroll") for (int i = 0; i < 4; ++i) _Pragma("unroll") for (int j = 0; j < 4; ++j) acc[i][j] = mfma(a[j], c[i], acc[i][j]); \
      acc8[0] = mfma(a[4], c[0], acc8[0]); acc8[1] = mfma(a[4], c[1], acc8[1]); } while (0)
#define ILV2() do { _Pragma("unroll") for (int z = 0; z < 9; ++z) { SGB(0x008, 1); SGB(0x100, 1); } SGB(0x008, 9); SCHED(); } while (0)
#define STEP2(slot) do { BAR(); RD2(c0, a0, slot, 0, oa0); MM2(c1, a1); ILV2(); RD2(c1, a1, slot, 1, oa1); MM2(c0, a0); ILV2(); WAITL0(); } while (0)
    RD2(c0, a0, 0, 0, oa0); RD2(c1, a1, 0, 1, oa1); MM2(c0, a0); SCHED(); WAITL0();
    WAITV(0);
    asm volatile("" :: "v"(warm));
    if (l == 0 && __hip_atomic_fetch_add(cnt + wa, 1u, __ATOMIC_RELAXED, __HIP_MEMORY_SCOPE_WORKGROUP) == 1u)
      __hip_atomic_store((GAS u64*)(p.ws + WS_F1) + ((panel * 2 + wa) * 16 + sig) * 16, E, __ATOMIC_RELAXED, __HIP_MEMORY_SCOPE_AGENT);
    STAMP(19);
    MM2(c1, a1); SCHED();
    BAR(); RD2(c0, a0, 1, 0, oa0); RD2(c1, a1, 1, 1, oa1); MM2(c0, a0); SCHED(); WAITL0();
    STAMP(4); STAMPC(28);
    STEP2(2); STEP2(3);
#pragma unroll 1
    for (int it = 0; it < 3; ++it) { STEP2(0); STEP2(1); STEP2(2); STEP2(3); }
    MM2(c1, a1);
    STAMP(5); STAMPC(29);
    const int t = l & 15, quad = l >> 4;
    const int pv = (t >> 3) * (16 * 2304) + quad * 128 + (t & 7) * 16, pv8 = (t >> 3) * (16 * 2304) + 2048 + quad * 64 + (t & 7) * 8;
#pragma unroll
    for (int i = 0; i < 4; ++i) {
      const int so = WS_PART + ((m * 16 + (wa * 4 + (i ^ (2 * wn))) * 2) * 16 + panel) * 2304;
#pragma unroll
      for (int u = 0; u < 2; ++u) {
        const f32x4 y0 = acc[i][2 * u], y1 = acc[i][2 * u + 1];
        __builtin_amdgcn_raw_buffer_store_b128(v4u{pack2(y0[0], y0[1]), pack2(y0[2], y0[3]), pack2(y1[0], y1[1]), pack2(y1[2], y1[3])}, rsw, pv, so + (wn * 2 + u) * 512, AUX_SC1);
      }
      if (i < 2) __builtin_amdgcn_raw_buffer_store_b64(v2u{pack2(acc8[i][0], acc8[i][1]), pack2(acc8[i][2], acc8[i][3])}, rsw, pv8, so, AUX_SC1);
    }
    STAMP(6);
    if (is_p) BAR();
    WAITV(0);
    asm volatile("" :: "v"(warm));
    STAMP(7);
    if (l == 0 && __hip_atomic_fetch_add(cnt + 2 + wa, 1u, __ATOMIC_RELAXED, __HIP_MEMORY_SCOPE_WORKGROUP) == 1u)
      __hip_atomic_store((GAS u64*)(p.ws + WS_F2) + ((m * 16 + panel) * 2 + wa) * 16, E, __ATOMIC_RELAXED, __HIP_MEMORY_SCOPE_AGENT);
    SCHED();
    if (!is_p) { BAR(); TOUCHW3(); }
    if (is_f) { LOADW_D(); SCHED(); LOADW_U(); SCHED(); LOADW_G(); SCHED(); }
    STAMP(20);
  }
  const unsigned tag = (unsigned)E;
  const rsrc_t rso = mkrs(p.out, S * D * 2);
  if (is_p) {
    LAS float* sst = (LAS float*)(L + L_SC);
    const int row = l & 7, quad = (l >> 3) & 3, half = l >> 5, nn = w & 3, rh = w >> 2;
    const int ea = (nn * 144 + 32 * half + 8 * quad) * 2, ee = (nn * 144 + 128 + 4 * quad + 2 * half) * 2, ro = (rh * 8 + row) * 1152;
    f32x2 h[9], wv[9];
    {
      const v4u xa = LDSV4(L + L_BR + ro + ea), xc = LDSV4(L + L_BR + ro + ea + 128), wa4 = LDSV4(L + L_WR + ea), wc4 = LDSV4(L + L_WR + ea + 128);
      const unsigned xe = *(LAS unsigned*)(L + L_BR + ro + ee), we = *(LAS unsigned*)(L + L_WR + ee);
#pragma unroll
      for (int i = 0; i < 4; ++i) { h[i] = unp(xa[i]); h[4 + i] = unp(xc[i]); wv[i] = unp(wa4[i]); wv[4 + i] = unp(wc4[i]); }
      h[8] = unp(xe); wv[8] = unp(we);
    }
    float sw;
    { const f32x2 t = ((wv[0] + wv[1]) + (wv[2] + wv[3])) + ((wv[4] + wv[5]) + (wv[6] + wv[7])) + wv[8]; sw = t[0] + t[1]; }
    sw += dppf<DPP_R8>(sw); sw = red16(sw, sw); sw = red32(sw, sw);
    const int pb = WS_PART + ((pi * 2 + rh) * 16 + nn) * 2304;
    GAS u64* f2 = (GAS u64*)(p.ws + WS_F2) + (((pi >> 3) * 16 + ((l >> 1) & 3) * 4 + nn) * 2 + ((pi >> 2) & 1)) * 16;
    v4u pa[4], pc[4]; unsigned pe[4];
    f32x2 pf[4][9];
    unsigned done = 0, conv = 0;
#define CONV(qq) do { _Pragma("unroll") for (int i = 0; i < 4; ++i) { pf[qq][i] = unp(pa[qq][i]); pf[qq][4 + i] = unp(pc[qq][i]); } pf[qq][8] = unp(pe[qq]); } while (0)
    for (unsigned sp = 0; sp < SPIN_CAP; ++sp) {
      const u64 v = __hip_atomic_load(f2, __ATOMIC_RELAXED, __HIP_MEMORY_SCOPE_AGENT);
      unsigned rdy = (unsigned)__builtin_amdgcn_ballot_w64(v == E);
      if (sp == SPIN_CAP - 1) rdy = 0x55u;
      asm volatile("" ::: "memory");
      const unsigned old_done = done;
#pragma unroll
      for (int qq = 0; qq < 4; ++qq)
        if ((rdy & ~done) >> (2 * qq) & 1) {
          pa[qq] = __builtin_amdgcn_raw_buffer_load_b128(rsw, l * 16, pb + qq * 4 * 2304, AUX_SC1);
          pc[qq] = __builtin_amdgcn_raw_buffer_load_b128(rsw, l * 16 + 1024, pb + qq * 4 * 2304, AUX_SC1);
          pe[qq] = __builtin_amdgcn_raw_buffer_load_b32(rsw, 2048 + quad * 64 + row * 8 + half * 4, pb + qq * 4 * 2304, AUX_SC1);
        }
      done |= rdy & 0x55u;
      if (done == 0x55u) break;
      if (done == old_done) {
#pragma unroll
        for (int qq = 0; qq < 4; ++qq) if ((done & ~conv) >> (2 * qq) & 1) CONV(qq);
        conv = done;
      }
    }
    STAMP(8);
#pragma unroll
    for (int qq = 0; qq < 4; ++qq) if (!(conv >> (2 * qq) & 1)) CONV(qq);
#pragma unroll
    for (int i = 0; i < 9; ++i) h[i] += (pf[0][i] + pf[1][i]) + (pf[2][i] + pf[3][i]);
    STAMP(9);
    float mu, m2, dt;
    {
      const f32x2 t = ((h[0] + h[1]) + (h[2] + h[3])) + ((h[4] + h[5]) + (h[6] + h[7])) + h[8];
      mu = (t[0] + t[1]) * (1.f / 18);
      f32x2 a = {0.f, 0.f}, c = {0.f, 0.f};
#pragma unroll
      for (int i = 0; i < 9; ++i) { const f32x2 d = h[i] - mu; a += d * d; c += h[i] * wv[i]; }
      m2 = a[0] + a[1]; dt = c[0] + c[1];
    }
    {
      const float mo = dppf<DPP_R8>(mu), d = mo - mu;
      m2 = m2 + dppf<DPP_R8>(m2) + d * d * 9.f; mu = 0.5f * (mu + mo); dt += dppf<DPP_R8>(dt);
      auto r = __builtin_amdgcn_permlane16_swap(asu(mu), asu(mu), false, false);
      float e = asf(r[0]) - asf(r[1]);
      m2 = red16(m2, m2) + e * e * 18.f; mu = 0.5f * (asf(r[0]) + asf(r[1])); dt = red16(dt, dt);
      auto r2 = __builtin_amdgcn_permlane32_swap(asu(mu), asu(mu), false, false);
      e = asf(r2[0]) - asf(r2[1]);
      m2 = red32(m2, m2) + e * e * 36.f; mu = 0.5f * (asf(r2[0]) + asf(r2[1])); dt = red32(dt, dt);
    }
    if (l < 8) *(LAS f32x4*)(sst + ((rh * 8 + row) * 4 + nn) * 4) = f32x4{mu, m2, dt, sw};
    WAITL0(); BAR();
    f32x4 st[4];
#pragma unroll
    for (int i = 0; i < 4; ++i) st[i] = *(LAS f32x4*)(sst + (((l & 15) ^ (8 * rh)) * 4 + i) * 4);
    const f32x4 tot = (st[0] + st[1]) + (st[2] + st[3]);
    float mean = tot[0] * 0.25f;
    float dv = 0;
#pragma unroll
    for (int i = 0; i < 4; ++i) { const float d = st[i][0] - mean; dv += d * d; }
    float rstd = __builtin_amdgcn_rsqf((tot[1] + 144.f * dv) * (1.f / D) + 1e-5f);
    const unsigned key = fkey(rstd * (tot[2] - mean * tot[3]));
    unsigned kmax = umax(key, dppu<DPP_X1>(key)); kmax = umax(kmax, dppu<DPP_X2>(kmax)); kmax = umax(kmax, dppu<DPP_HM>(kmax)); kmax = umax(kmax, dppu<DPP_R8>(kmax));
    unsigned hit = (unsigned)__builtin_amdgcn_ballot_w64(key == kmax) & 0xffffu;
    if (rh) hit = ((hit >> 8) | (hit << 8)) & 0xffffu;
    const int brow = __builtin_ctz(hit);
    if (tid < 8) __builtin_amdgcn_raw_buffer_store_b128(v4u{kmax, tag, (unsigned)(pi * 16 + brow), tag}, rsw, tid * 16384, WS_GRAN + pi * 128, AUX_SC1);
    { const float mo = dppf<DPP_R8>(mean), rs2 = dppf<DPP_R8>(rstd); if (l & 8) { mean = mo; rstd = rs2; } }
    f32x2 hn[9];
#pragma unroll
    for (int i = 0; i < 9; ++i) hn[i] = (h[i] - mean) * rstd;
    if (rh * 8 + row == brow) {
      const int co = nn * 576 + (4 * half + quad) * 32, cbase = WS_CAND + pi * 2304;
      __builtin_amdgcn_raw_buffer_store_b128(v4u{pk(hn[0]), tag, pk(hn[1]), tag}, rsw, co, cbase, AUX_SC1);
      __builtin_amdgcn_raw_buffer_store_b128(v4u{pk(hn[2]), tag, pk(hn[3]), tag}, rsw, co + 16, cbase, AUX_SC1);
      __builtin_amdgcn_raw_buffer_store_b128(v4u{pk(hn[4]), tag, pk(hn[5]), tag}, rsw, co + 256, cbase, AUX_SC1);
      __builtin_amdgcn_raw_buffer_store_b128(v4u{pk(hn[6]), tag, pk(hn[7]), tag}, rsw, co + 272, cbase, AUX_SC1);
      __builtin_amdgcn_raw_buffer_store_b64(v2u{pk(hn[8]), tag}, rsw, nn * 576 + 512 + (2 * quad + half) * 8, cbase, AUX_SC1);
    }
    STAMP(10);
#pragma unroll
    for (int i = 0; i < 9; ++i) h[i] += hn[i];
#define PUTOUT(AUX) do { \
      __builtin_amdgcn_raw_buffer_store_b128(v4u{pk(h[0]), pk(h[1]), pk(h[2]), pk(h[3])}, rso, ro + ea, pi * BSLOT, AUX); \
      __builtin_amdgcn_raw_buffer_store_b128(v4u{pk(h[4]), pk(h[5]), pk(h[6]), pk(h[7])}, rso, ro + ea + 128, pi * BSLOT, AUX); \
      __builtin_amdgcn_raw_buffer_store_b32(pk(h[8]), rso, ro + ee, pi * BSLOT, AUX); } while (0)
    PUTOUT(AUX_SC1);
    WAITV(0); BAR();
    STAMP(11);
    if (tid == 0) __hip_atomic_store((GAS u64*)(p.ws + WS_F3) + pi, E, __ATOMIC_RELAXED, __HIP_MEMORY_SCOPE_AGENT);
    STAMP_FLUSH();
    return;
  }
  if (!is_f && rb >= 36) { STAMP_FLUSH(); return; }
  unsigned mk[4], mz[4];
#pragma unroll
  for (int i = 0; i < 4; ++i) { mk[i] = ((l & 7) >> 1) == i ? ((l & 1) ? 0xffff0000u : 0xffffu) : 0u; mz[i] = (l & 8) ? 0u : mk[i]; asm volatile("" : "+v"(mk[i]), "+v"(mz[i])); }
  unsigned sel8 = (l & 8) ? 0x03020302u : 0x01000100u;
  int a_gw = L_SC + 1024 + (w * 128 + (l >> 5) * 64 + (l & 7) * 8 + ((l >> 4) & 1) * 4) * 4, a_gr = L_SC + 1024 + ((l & 7) * 128 + 8 * w + (l >> 3)) * 4;
  int a_yw = (w * 576 + (l & 15) * 8 + ((l >> 4) & 1) * 4) * 4;
  asm volatile("" : "+v"(sel8), "+v"(a_gw), "+v"(a_gr), "+v"(a_yw));
  LAS float* sy = (LAS float*)(L);
#define BEST(g0, g1, km, id) do { \
    const unsigned k0_ = (g0[1] == tag && g0[3] == tag) ? g0[0] : 0u, k1_ = (g1[1] == tag && g1[3] == tag) ? g1[0] : 0u; \
    const unsigned k_ = umax(k0_, k1_), i_ = k1_ > k0_ ? g1[2] : g0[2]; \
    km = umax(k_, dppu<DPP_X1>(k_)); km = umax(km, dppu<DPP_X2>(km)); km = umax(km, dppu<DPP_HM>(km)); km = umax(km, dppu<DPP_R8>(km)); \
    { auto r_ = __builtin_amdgcn_permlane16_swap(km, km, false, false); km = umax(r_[0], r_[1]); } \
    { auto r_ = __builtin_amdgcn_permlane32_swap(km, km, false, false); km = umax(r_[0], r_[1]); } \
    id = __builtin_amdgcn_readlane(i_, __builtin_ctzll(__builtin_amdgcn_ballot_w64(k_ == km))) & 2047; } while (0)
  if (is_f) {
    const int ci = 8 * (w & 1) + (l >> 3);
    v4u xa, xb; v2u xc;
    volatile LAS unsigned* sw = (volatile LAS unsigned*)(L + L_MISC + 1536 + 16);
    int have = -1;
    for (unsigned sp = 0; sp < 16 * SPIN_CAP; ++sp) {
      asm volatile("" ::: "memory");
      unsigned v;
      if (w == 4) {
        const v4u g0 = __builtin_amdgcn_raw_buffer_load_b128(rsw, l * 128, WS_GRAN + xcd * 16384, AUX_SC1);
        const v4u g1 = __builtin_amdgcn_raw_buffer_load_b128(rsw, l * 128 + 8192, WS_GRAN + xcd * 16384, AUX_SC1);
        unsigned km; int id;
        BEST(g0, g1, km, id);
        v = (unsigned)id | (__builtin_amdgcn_readfirstlane(km) ? 1u << 30 : 0u) | (__all(g0[1] == tag && g0[3] == tag && g1[1] == tag && g1[3] == tag) ? 3u << 30 : 0u);
        *sw = v;
      } else v = __builtin_amdgcn_readfirstlane(*sw);
      const int c16 = (v & 2047) >> 4;
      if ((v >> 30) && c16 != have) {
        const int cb = WS_CAND + c16 * 2304 + (w >> 1) * 576;
        xa = __builtin_amdgcn_raw_buffer_load_b128(rsw, ci * 32, cb, AUX_SC1);
        xb = __builtin_amdgcn_raw_buffer_load_b128(rsw, ci * 32 + 16, cb, AUX_SC1);
        xc = __builtin_amdgcn_raw_buffer_load_b64(rsw, 512 + (ci >> 1) * 8, cb, AUX_SC1);
        have = __all(xa[1] == tag && xa[3] == tag && xb[1] == tag && xb[3] == tag && xc[1] == tag) ? c16 : -1;
      }
      if ((v >> 31) && c16 == have) break;
      if (w == 4) __builtin_amdgcn_s_sleep(PSLEEP); else __builtin_amdgcn_s_sleep(1);
    }
    const unsigned xv[4] = {xa[0], xa[2], xb[0], xb[2]};
    STAMP(13);
    f32x4 ag = {0, 0, 0, 0}, au = {0, 0, 0, 0};
#pragma unroll
    for (int k = 0; k < 9; ++k) {
      const unsigned dup = k < 8 ? __builtin_amdgcn_perm(xv[k >> 1], xv[k >> 1], (k & 1) ? 0x03020302u : 0x01000100u) : __builtin_amdgcn_perm(xc[0], xc[0], sel8);
      const v4u A = {dup & mk[0], dup & mk[1], dup & mk[2], dup & mk[3]};
      ag = mfma(A, wg[k], ag); au = mfma(A, wu[k], au);
    }
    {
      f32x4 v4;
#pragma unroll
      for (int i = 0; i < 4; ++i) {
        const float gi = asf((unsigned)__builtin_amdgcn_update_dpp((int)asu(ag[i]), (int)asu(ag[i]), DPP_R8, 0xc, 0x3, false));
        const float ui = asf((unsigned)__builtin_amdgcn_update_dpp((int)asu(au[i]), (int)asu(au[i]), DPP_R8, 0xc, 0x3, false));
        v4[i] = red32(gi, ui);
      }
      if ((l & 8) == 0) *(LAS f32x4*)(L + a_gw) = v4;
    }
    WAITL0(); BAR();
    STAMP(14);
    float gg = *(LAS float*)(L + a_gr), uu = *(LAS float*)(L + a_gr + 256);
    gg += dppf<DPP_X1>(gg); uu += dppf<DPP_X1>(uu); gg += dppf<DPP_X2>(gg); uu += dppf<DPP_X2>(uu); gg += dppf<DPP_HM>(gg); uu += dppf<DPP_HM>(uu);
    const float gu = gg * __builtin_amdgcn_rcpf(1.f + __expf(-gg)) * uu;
    const unsigned d0 = pack2(gu, gu), d1 = dppu<DPP_R8>(d0);
    const v4u A0 = {d0 & mz[0], d0 & mz[1], d0 & mz[2], d0 & mz[3]}, A1 = {d1 & mz[0], d1 & mz[1], d1 & mz[2], d1 & mz[3]};
    f32x4 y[5];
#pragma unroll
    for (int hh = 0; hh < 5; ++hh) y[hh] = mfma(A0, wd[2 * hh], f32x4{0, 0, 0, 0});
#pragma unroll
    for (int hh = 0; hh < 5; ++hh) y[hh] = mfma(A1, wd[2 * hh + 1], y[hh]);
    if (l < 32) {
#pragma unroll
      for (int hh = 0; hh < 4; ++hh) *(LAS f32x4*)(L + a_yw + hh * 512) = y[hh];
      if ((l & 8) == 0) *(LAS f32x4*)(L + a_yw + 2048) = y[4];
    }
    WAITL0(); BAR();
    STAMP(15);
    if (tid < 288) {
      f32x2 v = *(LAS f32x2*)(sy + 2 * tid);
#pragma unroll
      for (int k = 1; k < 8; ++k) v += *(LAS f32x2*)(sy + k * 576 + 2 * tid);
      __builtin_amdgcn_raw_buffer_store_b128(v4u{asu(v[0]), 1u, asu(v[1]), 1u}, rsw, (tid >> 3) * 9216 + (tid & 7) * 16, WS_YG + fb * 128, AUX_SC1);
    }
  } else {
    float a0 = 0, a1 = 0;
    unsigned base_out = 0, wkey = 0;
    int wrow = 0;
    if (w == 0) {
      v4u g0, g1;
      for (unsigned sp = 0; sp < SPIN_CAP; ++sp) {
        asm volatile("" ::: "memory");
        g0 = __builtin_amdgcn_raw_buffer_load_b128(rsw, l * 128, WS_GRAN + xcd * 16384, AUX_SC1);
        g1 = __builtin_amdgcn_raw_buffer_load_b128(rsw, l * 128 + 8192, WS_GRAN + xcd * 16384, AUX_SC1);
        if (__all(g0[1] == tag && g0[3] == tag && g1[1] == tag && g1[3] == tag)) break;
        __builtin_amdgcn_s_sleep(8);
      }
      STAMP(12);
      BAR();
      BEST(g0, g1, wkey, wrow);
      for (unsigned sp = 0; sp < SPIN_CAP; ++sp) {
        if (__hip_atomic_load((GAS u64*)(p.ws + WS_F3) + (wrow >> 4), __ATOMIC_RELAXED, __HIP_MEMORY_SCOPE_AGENT) == E) break;
        __builtin_amdgcn_s_sleep(4);
      }
      asm volatile("" ::: "memory");
      if (tid < 8) base_out = __builtin_amdgcn_raw_buffer_load_b32(rso, tid * 4, wrow * 1152 + rb * 32, AUX_SC1);
    } else {
      BAR();
      __builtin_amdgcn_s_sleep(RSLEEP);
    }
    if (w >= 1 && w < 4) {
      v4u g[3];
      for (unsigned sp = 0; sp < SPIN_CAP; ++sp) {
        asm volatile("" ::: "memory");
#pragma unroll
        for (int k = 0; k < 3; ++k) g[k] = __builtin_amdgcn_raw_buffer_load_b128(rsw, l * 16 + k * 1024, WS_YG + rb * 9216 + (w - 1) * 3072, AUX_SC1);
        if (__all((g[0][1] & g[0][3] & g[1][1] & g[1][3] & g[2][1] & g[2][3]) == 1u)) break;
      }
#pragma unroll
      for (int k = 0; k < 3; ++k) { const unsigned t0 = g[k][0], t1 = g[k][2]; a0 += asf(t0); a1 += asf(t1); }
      float a = red32(a0, a1); a = red16(a, a); a += dppf<DPP_R8>(a);
      if ((l & 24) == 0) sy[(w - 1) * 16 + 2 * (l & 7) + (l >> 5)] = a;
      WAITL0();
    }
    BAR();
    STAMP(16);
    if (tid < 8) {
      const f32x2 y = *(LAS f32x2*)(sy + 2 * tid) + *(LAS f32x2*)(sy + 16 + 2 * tid) + *(LAS f32x2*)(sy + 32 + 2 * tid);
      const float sg = __builtin_amdgcn_rcpf(1.f + __expf(-unkey(wkey)));
      __builtin_amdgcn_raw_buffer_store_b32(pack2(bflo(base_out) + y[0] * sg, bfhi(base_out) + y[1] * sg), rso, tid * 4, wrow * 1152 + rb * 32, AUX_SC1);
      if (rb == 0 && tid == 0) __hip_atomic_store((GAS u64*)(p.ws + WS_EPOCH), E, __ATOMIC_RELAXED, __HIP_MEMORY_SCOPE_AGENT);
    }
  }
  STAMP(17);
  STAMP_FLUSH();
}

extern "C" void kernel_launch(void* const* d_in, const int* in_sizes, int n_in, void* d_out, int out_size, void* d_ws,
                              size_t ws_size, hipStream_t stream) {
  Params p;
  p.x = (const char*)d_in[0]; p.Cr = (const char*)d_in[1]; p.Ci = (const char*)d_in[2];
  p.Dr = (const char*)d_in[3]; p.Di = (const char*)d_in[4]; p.wr = (const char*)d_in[6];
  p.wg = (const char*)d_in[7]; p.wu = (const char*)d_in[8]; p.wd = (const char*)d_in[9];
  p.out = (char*)d_out; p.ws = (char*)d_ws;
  (void)hipFuncSetAttribute((const void*)fused, hipFuncAttributeMaxDynamicSharedMemorySize, LDS_BYTES);
  fused<<<256, 512, LDS_BYTES, stream>>>(p);
}
```
